```python
import torch

def _softmax(x, axis=-1):
    return torch.softmax(x, dim=axis)
def _gelu(x):
    return torch.nn.functional.gelu(x, approximate="tanh")
def _silu(x):
    return torch.nn.functional.silu(x)
def _sigmoid(x):
    return torch.sigmoid(x)
def _layernorm(x, axis=-1, eps=1e-5):
    m = x.mean(dim=axis, keepdim=True)
    v = x.var(dim=axis, keepdim=True, unbiased=False)
    return (x - m) / torch.sqrt(v + eps)
def _scatter_add(tgt, idx, src, axis=0):
    return tgt.clone().index_add_(axis, idx, src)
def _segment_sum(src, seg, n):
    out = torch.zeros((n,) + src.shape[1:], dtype=src.dtype)
    return out.index_add_(0, seg, src)
def _pad(x, pw):
    flat = tuple(v for pair in reversed(pw) for v in pair)
    return torch.nn.functional.pad(x, flat)

kernel_name = 'parallel_sum__mha_block_cached__glu_gated_bilinear__decode'

def setup_inputs():

    _w = (lambda a: (lambda t: t.to(torch.bfloat16) if t.is_floating_point() else t)(torch.as_tensor(a)))
    def _A_setup():
        B, Sq, D = 8, 1, 1024
        H, Sk, Dh = 32, 4096, 128
        return {
            "x": _w((lambda *s: torch.randn(*s, dtype=torch.bfloat16) * 0.05)(B, Sq, D)),
            "k_cache": _w((lambda *s: torch.randn(*s, dtype=torch.bfloat16) * 1.6)(B, H, Sk, Dh)),
            "v_cache": _w((lambda *s: torch.randn(*s, dtype=torch.bfloat16) * 0.8)(B, H, Sk, Dh)),
            "wq": _w((lambda *s: torch.randn(*s, dtype=torch.bfloat16) * 1.6)(D, H * Dh) * (D ** -0.5)),
            "wo": _w((lambda *s: torch.randn(*s, dtype=torch.bfloat16) * 0.8)(H * Dh, D) * ((H * Dh) ** -0.5)),
        }
    _A_in = _A_setup()

    def _B_setup():
        B, S, D, F = 8, 1, 1024, 3584
        return {
            "x": _w((lambda *s: torch.randn(*s, dtype=torch.bfloat16) * 0.05)(B, S, D)),
            "w_gate": _w((lambda *s: torch.randn(*s, dtype=torch.bfloat16) * 0.4)(D, F) * (D ** -0.5)),
            "w_up": _w((lambda *s: torch.randn(*s, dtype=torch.bfloat16) * 0.4)(D, F) * (D ** -0.5)),
            "w_down": _w((lambda *s: torch.randn(*s, dtype=torch.bfloat16) * 0.4)(F, D) * (F ** -0.5)),
        }
    _B_in = _B_setup()

    return {
        "A_x": _A_in["x"],
        "A_k_cache": _A_in["k_cache"],
        "A_v_cache": _A_in["v_cache"],
        "A_wq": _A_in["wq"],
        "A_wo": _A_in["wo"],
        "B_x": _B_in["x"],
        "B_w_gate": _B_in["w_gate"],
        "B_w_up": _B_in["w_up"],
        "B_w_down": _B_in["w_down"],
    }

def reference(A_x, A_k_cache, A_v_cache, A_wq, A_wo, B_x, B_w_gate, B_w_up, B_w_down):
    def _A_ref(x, k_cache, v_cache, wq, wo):
        B, Sq, D = x.shape
        H, Dh = 32, 128
        q = (lambda x, *d: x.permute(*d))((x @ wq).reshape(B, Sq, H, Dh), 0, 2, 1, 3)
        scores = torch.einsum("bhqd,bhkd->bhqk", q, k_cache) * (Dh ** -0.5)
        p = _softmax(scores, axis=-1)
        o = torch.einsum("bhqk,bhkd->bhqd", p, v_cache)
        return (lambda x, *d: x.permute(*d))(o, 0, 2, 1, 3).reshape(B, Sq, H * Dh) @ wo

    def _B_ref(x, w_gate, w_up, w_down):
        g = torch.einsum("bsd,df->bsf", x, w_gate)
        u = torch.einsum("bsd,df->bsf", x, w_up)
        h = g * u
        return torch.einsum("bsf,fd->bsd", h, w_down)

    x = A_x
    xn = _layernorm(x, axis=-1)
    return x + _A_ref(xn, A_k_cache, A_v_cache, A_wq, A_wo) + _B_ref(xn, B_w_gate, B_w_up, B_w_down)
```

```cpp
#include <hip/hip_runtime.h>
#include <hip/hip_bf16.h>
#include <stdint.h>

typedef unsigned u32x4 __attribute__((ext_vector_type(4)));
typedef float f32x4 __attribute__((ext_vector_type(4)));
typedef __bf16 bf16x2 __attribute__((ext_vector_type(2)));

constexpr int B_ = 8, D_ = 1024, H_ = 32, SK = 4096, DH = 128, F_ = 3584, HD = H_ * DH;

__device__ __forceinline__ float bflo(unsigned x) { return __builtin_bit_cast(float, x << 16); }
__device__ __forceinline__ float bfhi(unsigned x) { return __builtin_bit_cast(float, x & 0xffff0000u); }
__device__ __forceinline__ float rbf(float x) { return (float)(__bf16)x; }
__device__ __forceinline__ unsigned short tobf(float x) { return __builtin_bit_cast(unsigned short, (__bf16)x); }
__device__ __forceinline__ float dot2(unsigned k, unsigned q, float acc) {
  return __builtin_amdgcn_fdot2_f32_bf16(__builtin_bit_cast(bf16x2, k), __builtin_bit_cast(bf16x2, q), acc, false);
}
template <int CTRL> __device__ __forceinline__ float dpp(float x) {
  return __builtin_bit_cast(float, __builtin_amdgcn_mov_dpp(__builtin_bit_cast(int, x), CTRL, 0xf, 0xf, true));
}
constexpr int XOR1 = 0xB1, XOR2 = 0x4E, XOR7 = 0x141, XOR8 = 0x128;
__device__ __forceinline__ float xrow_max(float x) {
  auto s = __builtin_amdgcn_permlane16_swap(__float_as_uint(x), __float_as_uint(x), false, false);
  x = fmaxf(__uint_as_float(s[0]), __uint_as_float(s[1]));
  auto t = __builtin_amdgcn_permlane32_swap(__float_as_uint(x), __float_as_uint(x), false, false);
  return fmaxf(__uint_as_float(t[0]), __uint_as_float(t[1]));
}
__device__ __forceinline__ float xrow_sum(float x) {
  auto s = __builtin_amdgcn_permlane16_swap(__float_as_uint(x), __float_as_uint(x), false, false);
  x = __uint_as_float(s[0]) + __uint_as_float(s[1]);
  auto t = __builtin_amdgcn_permlane32_swap(__float_as_uint(x), __float_as_uint(x), false, false);
  return __uint_as_float(t[0]) + __uint_as_float(t[1]);
}
__device__ __forceinline__ float wave_sum(float x) {
  x += dpp<XOR1>(x); x += dpp<XOR2>(x); x += dpp<XOR7>(x); x += dpp<XOR8>(x); return xrow_sum(x);
}
__device__ __forceinline__ float wave_max(float x) {
  x = fmaxf(x, dpp<XOR1>(x)); x = fmaxf(x, dpp<XOR2>(x)); x = fmaxf(x, dpp<XOR7>(x)); x = fmaxf(x, dpp<XOR8>(x)); return xrow_max(x);
}
__device__ __forceinline__ u32x4 ldnt(const char* p) { return __builtin_nontemporal_load((const u32x4*)p); }

__device__ __forceinline__ void ln_rows(const unsigned short* x, float* xs, int wave, int lane) {
  if (wave < B_) {
    const u32x4* xr = (const u32x4*)(x + wave * D_);
    u32x4 a = xr[lane], b = xr[64 + lane];
    float v[16];
#pragma unroll
    for (int j = 0; j < 4; ++j) { v[2 * j] = bflo(a[j]); v[2 * j + 1] = bfhi(a[j]); v[8 + 2 * j] = bflo(b[j]); v[8 + 2 * j + 1] = bfhi(b[j]); }
    float s = 0.f;
#pragma unroll
    for (int j = 0; j < 16; ++j) s += v[j];
    const float mean = wave_sum(s) * (1.f / D_);
    float q = 0.f;
#pragma unroll
    for (int j = 0; j < 16; ++j) { const float d = v[j] - mean; q = fmaf(d, d, q); }
    const float var = wave_sum(q) * (1.f / D_);
    const float mb = rbf(mean), sd = rbf(sqrtf(rbf(rbf(var) + 1e-5f)));
#pragma unroll
    for (int j = 0; j < 16; ++j) { const int k = (j < 8 ? 0 : 512) + lane * 8 + (j & 7); xs[k * 8 + wave] = rbf(rbf(v[j] - mb) / sd); }
  }
}

__device__ __forceinline__ void gemv32(const unsigned short* W, int ldw, int col0, int r0, int nrows, const float* as, float (&acc)[64], int wave, int lane) {
  for (int r = wave * 16 + (lane >> 2); r < nrows; r += 128) {
    const u32x4 w = ldnt((const char*)(W + (size_t)(r0 + r) * ldw + col0 + 8 * (lane & 3)));
    const f32x4 a0 = *(const f32x4*)(as + r * 8), a1 = *(const f32x4*)(as + r * 8 + 4);
#pragma unroll
    for (int j = 0; j < 4; ++j) {
      const float lo = bflo(w[j]), hi = bfhi(w[j]);
#pragma unroll
      for (int b = 0; b < 4; ++b) {
        acc[b * 8 + 2 * j] = fmaf(a0[b], lo, acc[b * 8 + 2 * j]); acc[b * 8 + 2 * j + 1] = fmaf(a0[b], hi, acc[b * 8 + 2 * j + 1]);
        acc[(b + 4) * 8 + 2 * j] = fmaf(a1[b], lo, acc[(b + 4) * 8 + 2 * j]); acc[(b + 4) * 8 + 2 * j + 1] = fmaf(a1[b], hi, acc[(b + 4) * 8 + 2 * j + 1]);
      }
    }
  }
}
__device__ __forceinline__ float reduce32(float (&acc)[64], float* red, int wave, int lane, int tid) {
#pragma unroll
  for (int i = 0; i < 64; ++i) { float t = acc[i]; t += dpp<0x124>(t); t += dpp<XOR8>(t); acc[i] = xrow_sum(t); }
  if (lane < 4) {
#pragma unroll
    for (int i = 0; i < 64; ++i) red[wave * 256 + (i >> 3) * 32 + lane * 8 + (i & 7)] = acc[i];
  }
  __syncthreads();
  float s = 0.f;
  if (tid < 256) {
#pragma unroll
    for (int w = 0; w < 8; ++w) s += red[w * 256 + tid];
  }
  __syncthreads();
  return s;
}

__global__ void __launch_bounds__(512) k_pre(const unsigned short* x, const unsigned short* wq, const unsigned short* wg, const unsigned short* wu,
                                              unsigned short* qout, float* hout) {
  __shared__ __attribute__((aligned(16))) float xs[D_ * 8];
  __shared__ float red[8 * 256];
  const int tid = threadIdx.x, wave = tid >> 6, lane = tid & 63;
  ln_rows(x, xs, wave, lane);
  __syncthreads();
  float acc[64];
#pragma unroll
  for (int i = 0; i < 64; ++i) acc[i] = 0.f;
  if (blockIdx.x < HD / 32) {
    const int col0 = blockIdx.x * 32;
    gemv32(wq, HD, col0, 0, D_, xs, acc, wave, lane);
    const float s = reduce32(acc, red, wave, lane, tid);
    if (tid < 256) qout[(tid >> 5) * HD + col0 + (tid & 31)] = tobf(s);
  } else {
    const int col0 = (blockIdx.x - HD / 32) * 32;
    gemv32(wg, F_, col0, 0, D_, xs, acc, wave, lane);
    const float g = reduce32(acc, red, wave, lane, tid);
#pragma unroll
    for (int i = 0; i < 64; ++i) acc[i] = 0.f;
    gemv32(wu, F_, col0, 0, D_, xs, acc, wave, lane);
    const float u = reduce32(acc, red, wave, lane, tid);
    if (tid < 256) hout[(col0 + (tid & 31)) * 8 + (tid >> 5)] = rbf(rbf(g) * rbf(u));
  }
}

template <int N>
__global__ void __launch_bounds__(256) k_attn(const unsigned short* q, const char* __restrict__ K, const char* __restrict__ V, float* oout) {
  __shared__ __attribute__((aligned(16))) float mrg[4 * 132];
  const int tid = threadIdx.x, lane = tid & 63, wave = __builtin_amdgcn_readfirstlane(tid >> 6), c = lane & 15;
  const int row = blockIdx.x;
  const __amdgpu_buffer_rsrc_t rk = __builtin_amdgcn_make_buffer_rsrc((void*)(K + (size_t)row * (SK * DH * 2)), 0, SK * DH * 2, 0x00020000);
  const __amdgpu_buffer_rsrc_t rv = __builtin_amdgcn_make_buffer_rsrc((void*)(V + (size_t)row * (SK * DH * 2)), 0, SK * DH * 2, 0x00020000);
  const int voff = wave * 4096 + lane * 16;
  int soff = 0;
  const u32x4 qv = *(const u32x4*)(q + (size_t)row * DH + 8 * c);
  u32x4 a[N][4];
  float acc[8], m = -INFINITY, l = 0.f;
#pragma unroll
  for (int k = 0; k < 8; ++k) acc[k] = 0.f;
  const bool b3 = c & 8, b2 = c & 4, b1 = c & 2, b0 = c & 1;
  constexpr float SCALE = 0.08838834764831845f, LOG2E = 1.4426950408889634f;
#define ISSUE(n, t) _Pragma("unroll") for (int g_ = 0; g_ < 4; ++g_) { \
    a[n][g_] = __builtin_amdgcn_raw_buffer_load_b128(((t) & 8) ? rv : rk, voff + g_ * 1024, soff + ((t) & 7) * 16384 + ((t) >> 4) * 131072, 2); __builtin_amdgcn_sched_barrier(0); }
#define ADDR(t) (t)
#pragma unroll
  for (int n = 0; n < N; ++n) ISSUE(n, ADDR(n))
  auto block = [&](auto last_tag) {
    constexpr bool LAST = decltype(last_tag)::value;
    float z[4], s[2], p[2];
#pragma unroll
    for (int t = 0; t < 8; ++t) {
      float x[4];
#pragma unroll
      for (int i = 0; i < 4; ++i) { float d = 0.f;
#pragma unroll
        for (int k = 0; k < 4; ++k) d = dot2(a[t % N][i][k], qv[k], d);
        x[i] = d; }
      asm volatile("" : "+v"(x[0]), "+v"(x[1]), "+v"(x[2]), "+v"(x[3]));
      __builtin_amdgcn_sched_barrier(0);
      ISSUE(t % N, ADDR(t + N))
      const float y0 = (b3 ? x[2] : x[0]) + dpp<XOR8>(b3 ? x[0] : x[2]), y1 = (b3 ? x[3] : x[1]) + dpp<XOR8>(b3 ? x[1] : x[3]);
      z[t & 3] = (b2 ? y1 : y0) + dpp<XOR7>(b2 ? y0 : y1);
      if ((t & 3) == 3) {
        const float u0 = (b1 ? z[2] : z[0]) + dpp<XOR2>(b1 ? z[0] : z[2]), u1 = (b1 ? z[3] : z[1]) + dpp<XOR2>(b1 ? z[1] : z[3]);
        s[t >> 2] = (b0 ? u1 : u0) + dpp<XOR1>(b0 ? u0 : u1);
      }
    }
#pragma unroll
    for (int j = 0; j < 2; ++j) s[j] = rbf(rbf(s[j]) * SCALE) * LOG2E;
    const float mn = fmaxf(m, wave_max(fmaxf(s[0], s[1])));
    const float alpha = __builtin_amdgcn_exp2f(m - mn);
    m = mn;
    p[0] = __builtin_amdgcn_exp2f(s[0] - mn); p[1] = __builtin_amdgcn_exp2f(s[1] - mn);
    l = fmaf(l, alpha, p[0] + p[1]);
#pragma unroll
    for (int k = 0; k < 8; ++k) acc[k] *= alpha;
#define PV(t, i) { const float pb = dpp<0x150 + 4 * (i) + ((t) & 3)>(p[((t) >> 2) & 1]); \
      _Pragma("unroll") for (int k = 0; k < 4; ++k) { acc[2 * k] = fmaf(pb, bflo(a[(t) % N][i][k]), acc[2 * k]); acc[2 * k + 1] = fmaf(pb, bfhi(a[(t) % N][i][k]), acc[2 * k + 1]); } }
#define VGRP(t) { PV(t, 0) PV(t, 1) PV(t, 2) PV(t, 3) \
      asm volatile("" : "+v"(acc[0]), "+v"(acc[1]), "+v"(acc[2]), "+v"(acc[3]), "+v"(acc[4]), "+v"(acc[5]), "+v"(acc[6]), "+v"(acc[7])); \
      __builtin_amdgcn_sched_barrier(0); \
      if (!LAST || (t) + N < 16) ISSUE((t) % N, ADDR((t) + N)) }
    VGRP(8) VGRP(9) VGRP(10) VGRP(11) VGRP(12) VGRP(13) VGRP(14) VGRP(15)
    soff += 131072;
  };
  for (int blk = 0; blk < 7; ++blk) block(std::false_type{});
  block(std::true_type{});
#pragma unroll
  for (int k = 0; k < 8; ++k) acc[k] = xrow_sum(acc[k]);
  l = wave_sum(l);
  if (lane < 16) {
    *(f32x4*)(mrg + wave * 132 + 4 + 8 * c) = f32x4{acc[0], acc[1], acc[2], acc[3]};
    *(f32x4*)(mrg + wave * 132 + 8 + 8 * c) = f32x4{acc[4], acc[5], acc[6], acc[7]};
    if (lane == 0) { mrg[wave * 132] = m; mrg[wave * 132 + 1] = l; }
  }
  __syncthreads();
  if (tid < DH) {
    const float M = fmaxf(fmaxf(mrg[0], mrg[132]), fmaxf(mrg[264], mrg[396]));
    float L = 0.f, o = 0.f;
#pragma unroll
    for (int w = 0; w < 4; ++w) { const float e = __builtin_amdgcn_exp2f(mrg[w * 132] - M); L = fmaf(e, mrg[w * 132 + 1], L); o = fmaf(e, mrg[w * 132 + 4 + tid], o); }
    oout[((row & 31) * DH + tid) * 8 + (row >> 5)] = rbf(o / L);
  }
}

__global__ void __launch_bounds__(512) k_fin(const float* o, const float* h, const unsigned short* wo, const unsigned short* wd, float* part) {
  __shared__ __attribute__((aligned(16))) float as[1024 * 8];
  __shared__ float red[8 * 256];
  const int tid = threadIdx.x, wave = tid >> 6, lane = tid & 63;
  const int cb = blockIdx.x & 31, sp = blockIdx.x >> 5;
  const int nrows = sp < 4 ? 1024 : 896, r0 = sp < 4 ? sp * 1024 : (sp - 4) * 896;
  const float* src = (sp < 4 ? o : h) + (size_t)r0 * 8;
  for (int i = tid; i < nrows * 2; i += 512) ((f32x4*)as)[i] = ((const f32x4*)src)[i];
  __syncthreads();
  float acc[64];
#pragma unroll
  for (int i = 0; i < 64; ++i) acc[i] = 0.f;
  gemv32(sp < 4 ? wo : wd, D_, cb * 32, r0, nrows, as, acc, wave, lane);
  const float s = reduce32(acc, red, wave, lane, tid);
  if (tid < 256) part[(sp * 8 + (tid >> 5)) * D_ + cb * 32 + (tid & 31)] = s;
}

__global__ void __launch_bounds__(256) k_sum(const float* part, const unsigned short* x, unsigned short* out) {
  const int i = blockIdx.x * 256 + threadIdx.x;
  float a = 0.f, f = 0.f;
#pragma unroll
  for (int s = 0; s < 4; ++s) { a += part[s * 8192 + i]; f += part[(4 + s) * 8192 + i]; }
  const float xv = __builtin_bit_cast(float, (unsigned)x[i] << 16);
  out[i] = tobf(rbf(xv + rbf(a)) + rbf(f));
}

extern "C" void kernel_launch(void* const* d_in, const int* in_sizes, int n_in, void* d_out, int out_size, void* d_ws, size_t ws_size,
                              hipStream_t stream) {
  const unsigned short* x = (const unsigned short*)d_in[0];
  const char* K = (const char*)d_in[1];
  const char* V = (const char*)d_in[2];
  const unsigned short* wq = (const unsigned short*)d_in[3];
  const unsigned short* wo = (const unsigned short*)d_in[4];
  const unsigned short* wg = (const unsigned short*)d_in[6];
  const unsigned short* wu = (const unsigned short*)d_in[7];
  const unsigned short* wd = (const unsigned short*)d_in[8];
  char* ws = (char*)d_ws;
  unsigned short* q = (unsigned short*)ws;
  float* h = (float*)(ws + (1 << 16));
  float* o = (float*)(ws + (1 << 18));
  float* part = (float*)(ws + (1 << 19));
  k_pre<<<HD / 32 + F_ / 32, 512, 0, stream>>>(x, wq, wg, wu, q, h);
  k_attn<4><<<256, 256, 0, stream>>>(q, K, V, o);
  k_fin<<<256, 512, 0, stream>>>(o, h, wo, wd, part);
  k_sum<<<32, 256, 0, stream>>>(part, x, (unsigned short*)d_out);
}
```

```cpp
#include <hip/hip_runtime.h>
#include <hip/hip_bf16.h>
#include <stdint.h>
#include <type_traits>

typedef unsigned u32x4 __attribute__((ext_vector_type(4)));
typedef unsigned u32x2 __attribute__((ext_vector_type(2)));
typedef float f32x4 __attribute__((ext_vector_type(4)));
typedef float f32x2 __attribute__((ext_vector_type(2)));
typedef short s16x4 __attribute__((ext_vector_type(4)));
typedef short s16x8 __attribute__((ext_vector_type(8)));
typedef __bf16 bf16x2 __attribute__((ext_vector_type(2)));
typedef __bf16 bf16x8 __attribute__((ext_vector_type(8)));
typedef unsigned long long u64;
#define GAS __attribute__((address_space(1)))
#define LAS __attribute__((address_space(3)))
#define RLX_AGENT __ATOMIC_RELAXED, __HIP_MEMORY_SCOPE_AGENT
#define RLX_WG __ATOMIC_RELAXED, __HIP_MEMORY_SCOPE_WORKGROUP

constexpr int D_ = 1024, SK = 4096, DH = 128, F_ = 3584, HD = 4096;
constexpr int NTHR = 768;
#define NBLK_ 7
constexpr int NBLK = NBLK_;
constexpr int IPR = (8 - NBLK) * 4;
constexpr int NITEMS = 8 * IPR;
static_assert((NITEMS & (NITEMS - 1)) == 0 && NBLK * 512 + IPR * 128 == SK, "item split");
constexpr int NSLOT = 1 + IPR;
constexpr unsigned SPIN_CAP = 1u << 17;
#define WQ_AUX 2
#define W_AUX 2
#define GU_AUX 2
#define S_PRIO 0
#define H_PRIO 2
#define END_SLEEP 8
#define PROG_SLEEP 2
#define WAIT_G2 2
#define WAIT_Q 3
#define WAIT_H 4
#define STAGGER 16
#define SKEW -1
#define DIST 3
#define W_EARLY 8

constexpr int L_XN = 0, XN_STR = 2064;
constexpr int L_QP = L_XN + 8 * XN_STR;
constexpr int L_GUP = L_QP + 4096;
constexpr int L_QG = L_GUP + 16384;
constexpr int L_DUMP = L_QG + 2048, DUMP_REC = 528;
constexpr int L_W = L_DUMP + 16 * DUMP_REC, W_TS = 8224;
constexpr int L_OA = L_W + 8 * W_TS, A_STR = 272;
constexpr int L_HA = L_OA + 8 * A_STR;
constexpr int L_CTL = L_HA + 8 * A_STR;
constexpr int C_TICK = 64, C_P = 44, C_QG = 48, C_CNT = 49, C_GU = 53, C_WC = 54, C_OA = 55, C_END = 56, C_SDONE = 57, C_TAG = 58, C_ST = 59, C_K23 = 60, C_GI = 61, C_BLK = 62, C_Q = 63, C_WORDS = 64 + 128;
constexpr int L_BYTES = L_CTL + C_WORDS * 4;
constexpr unsigned W_EPOCH = 0, W_CNT = 4096, W_QG = 16384, W_HG = W_QG + 256 * 512, W_LSE = W_HG + 32 * 4096, W_REC = W_LSE + 256 * 256,
                   W_PART = W_REC + 256 * NSLOT * 512, W_QS = W_PART + 256 * 4096, W_GS = W_QS + 256 * 4096, W_END = W_GS + 224 * 4096, W_STAMP = 8u << 20;
constexpr int OOB = 0x7ffffff0;

__device__ __forceinline__ float bflo(unsigned x) { return __builtin_bit_cast(float, x << 16); }
__device__ __forceinline__ float bfhi(unsigned x) { return __builtin_bit_cast(float, x & 0xffff0000u); }
__device__ __forceinline__ float rbf(float x) { return (float)(__bf16)x; }
__device__ __forceinline__ unsigned pack2(float lo, float hi) { bf16x2 v = {(__bf16)lo, (__bf16)hi}; return __builtin_bit_cast(unsigned, v); }
__device__ __forceinline__ float dot2(unsigned k, unsigned q, float acc) {
  return __builtin_amdgcn_fdot2_f32_bf16(__builtin_bit_cast(bf16x2, k), __builtin_bit_cast(bf16x2, q), acc, false);
}
template <int CTRL> __device__ __forceinline__ float dpp(float x) {
  return __builtin_bit_cast(float, __builtin_amdgcn_mov_dpp(__builtin_bit_cast(int, x), CTRL, 0xf, 0xf, true));
}
constexpr int XOR1 = 0xB1, XOR2 = 0x4E, XOR7 = 0x141, XOR8 = 0x128;
__device__ __forceinline__ float xrow_max(float x) {
  auto s = __builtin_amdgcn_permlane16_swap(__float_as_uint(x), __float_as_uint(x), false, false);
  x = fmaxf(__uint_as_float(s[0]), __uint_as_float(s[1]));
  auto t = __builtin_amdgcn_permlane32_swap(__float_as_uint(x), __float_as_uint(x), false, false);
  return fmaxf(__uint_as_float(t[0]), __uint_as_float(t[1]));
}
__device__ __forceinline__ float x32_sum(float x) {
  auto t = __builtin_amdgcn_permlane32_swap(__float_as_uint(x), __float_as_uint(x), false, false);
  return __uint_as_float(t[0]) + __uint_as_float(t[1]);
}
__device__ __forceinline__ float xrow_sum(float x) {
  auto s = __builtin_amdgcn_permlane16_swap(__float_as_uint(x), __float_as_uint(x), false, false);
  return x32_sum(__uint_as_float(s[0]) + __uint_as_float(s[1]));
}
__device__ __forceinline__ float wave_sum(float x) {
  x += dpp<XOR1>(x); x += dpp<XOR2>(x); x += dpp<XOR7>(x); x += dpp<XOR8>(x); return xrow_sum(x);
}
__device__ __forceinline__ float wave_max(float x) {
  x = fmaxf(x, dpp<XOR1>(x)); x = fmaxf(x, dpp<XOR2>(x)); x = fmaxf(x, dpp<XOR7>(x)); x = fmaxf(x, dpp<XOR8>(x)); return xrow_max(x);
}
__device__ __forceinline__ void lgkm0() { asm volatile("s_waitcnt lgkmcnt(0)" ::: "memory"); }
__device__ __forceinline__ void cfence() { asm volatile("" ::: "memory"); }
__device__ __forceinline__ void wg_barrier() { asm volatile("s_waitcnt lgkmcnt(0)\n\ts_barrier" ::: "memory"); }
__device__ __forceinline__ int lds_ld(LAS int* p) { return __builtin_amdgcn_readfirstlane(__hip_atomic_load(p, RLX_WG)); }
__device__ __forceinline__ int lds_add(LAS int* p) {
  int old; u64 keep;
  asm volatile("s_waitcnt lgkmcnt(0)\n\ts_mov_b64 %1, exec\n\ts_mov_b64 exec, 1\n\tds_add_rtn_u32 %0, %2, %3\n\ts_mov_b64 exec, %1\n\ts_waitcnt lgkmcnt(0)"
               : "=&v"(old), "=&s"(keep) : "v"(p), "v"(1) : "memory");
  return __builtin_amdgcn_readfirstlane(old);
}
template <int SLEEP> __device__ __forceinline__ void lds_wait_ge(LAS int* p, int want) {
  for (unsigned s = 0; lds_ld(p) < want && s < (SPIN_CAP << 4); ++s) __builtin_amdgcn_s_sleep(SLEEP);
  cfence();
}
__device__ __forceinline__ unsigned tagw(float v, unsigned tag) { return ((__float_as_uint(v) + 0x80u) & 0xffffff00u) | tag; }
__device__ __forceinline__ float untag(unsigned w) { return __uint_as_float(w & 0xffffff00u); }
__device__ __forceinline__ void st_tag4(__amdgpu_buffer_rsrc_t rw, int off, f32x4 v, unsigned tag) {
  __builtin_amdgcn_raw_buffer_store_b128(u32x4{tagw(v[0], tag), tagw(v[1], tag), tagw(v[2], tag), tagw(v[3], tag)}, rw, off, 0, 16);
}
__device__ __forceinline__ u32x4 ld_tag4(__amdgpu_buffer_rsrc_t rw, int off) { return __builtin_amdgcn_raw_buffer_load_b128(rw, off, 0, 16); }
__device__ __forceinline__ bool tag_ok(u32x4 w, unsigned tag) { return (((w[0] ^ tag) | (w[1] ^ tag) | (w[2] ^ tag) | (w[3] ^ tag)) & 0xffu) == 0u; }
__device__ __forceinline__ f32x4 untag4(u32x4 w) { return f32x4{untag(w[0]), untag(w[1]), untag(w[2]), untag(w[3])}; }
__device__ __forceinline__ u32x2 hi16x4(u32x4 w) { return u32x2{__builtin_amdgcn_perm(w[1], w[0], 0x07060302), __builtin_amdgcn_perm(w[3], w[2], 0x07060302)}; }
__device__ __forceinline__ void nap(int n) { for (int i = 0; i < n; ++i) __builtin_amdgcn_s_sleep(16); }
__device__ __forceinline__ int rowpos(int row) { return row ^ ((row >> 1) & 4); }
__device__ __forceinline__ s16x4 ld_tr(LAS char* p) { return __builtin_amdgcn_ds_read_tr16_b64_v4i16((LAS s16x4*)p); }
__device__ __forceinline__ bf16x8 ld_bfrag(LAS char* img, int row0, int lane) {
  const int g = lane >> 4, q = (lane >> 2) & 3, p = lane & 3;
  const s16x4 lo = ld_tr(img + rowpos(row0 + 8 * g + q) * 32 + p * 8), hi = ld_tr(img + rowpos(row0 + 8 * g + 4 + q) * 32 + p * 8);
  return __builtin_bit_cast(bf16x8, __builtin_shufflevector(lo, hi, 0, 1, 2, 3, 4, 5, 6, 7));
}
#define STAMP_AT(i) do { } while (0)
#define STAMP_VAL(i, v) do { } while (0)
#define STAMP_DUMP() do { } while (0)
#define L_EXTRA 0

__device__ __forceinline__ void ln_row(const u32x4 xa, const u32x4 xb, LAS char* dst, int lane) {
  float v[16];
#pragma unroll
  for (int j = 0; j < 4; ++j) { v[2 * j] = bflo(xa[j]); v[2 * j + 1] = bfhi(xa[j]); v[8 + 2 * j] = bflo(xb[j]); v[8 + 2 * j + 1] = bfhi(xb[j]); }
  float s = 0.f;
#pragma unroll
  for (int j = 0; j < 16; ++j) s += v[j];
  const float mean = wave_sum(s) * (1.f / D_);
  float q = 0.f;
#pragma unroll
  for (int j = 0; j < 16; ++j) { const float d = v[j] - mean; q = fmaf(d, d, q); }
  const float var = wave_sum(q) * (1.f / D_);
  const float mb = rbf(mean), rs = 1.f / rbf(sqrtf(rbf(rbf(var) + 1e-5f)));
  u32x4 o0, o1;
#pragma unroll
  for (int j = 0; j < 4; ++j) {
    o0[j] = pack2(rbf(v[2 * j] - mb) * rs, rbf(v[2 * j + 1] - mb) * rs);
    o1[j] = pack2(rbf(v[8 + 2 * j] - mb) * rs, rbf(v[8 + 2 * j + 1] - mb) * rs);
  }
  *(LAS u32x4*)(dst + lane * 16) = o0;
  *(LAS u32x4*)(dst + 1024 + lane * 16) = o1;
}

__global__ void __launch_bounds__(NTHR, 3)
fused_decode(const unsigned short* __restrict__ x, const char* __restrict__ Kc, const char* __restrict__ Vc, const char* __restrict__ wq,
             const char* __restrict__ wo, const char* __restrict__ wg, const char* __restrict__ wu, const char* __restrict__ wd,
             unsigned short* __restrict__ out, char* ws) {
  __shared__ __attribute__((aligned(16))) char smem_[L_BYTES + L_EXTRA];
  LAS char* const sm = (LAS char*)smem_;
  LAS int* const ctl = (LAS int*)(sm + L_CTL);
  const int tid = threadIdx.x, lane = tid & 63, wid = __builtin_amdgcn_readfirstlane(tid >> 6), r = lane >> 4, c = lane & 15;
  const int xcd = blockIdx.x & 7, grp = blockIdx.x >> 3;
  const int head = 4 * xcd + (grp & 3), bat = grp >> 2;

  const __amdgpu_buffer_rsrc_t rw = __builtin_amdgcn_make_buffer_rsrc((void*)ws, 0, W_END, 0x00020000);

  if (wid < 4) {
    const int wave = wid;
    const __amdgpu_buffer_rsrc_t rk = __builtin_amdgcn_make_buffer_rsrc((void*)Kc, 0, 1 << 28, 0x00020000);
    const __amdgpu_buffer_rsrc_t rv = __builtin_amdgcn_make_buffer_rsrc((void*)Vc, 0, 1 << 28, 0x00020000);
    const int voff = wave * 4096 + lane * 16;
    int soff = (bat * 32 + head) << 20;
    u32x4 a[4][4];
#define ISSUE(n, rs, so) _Pragma("unroll") for (int g_ = 0; g_ < 4; ++g_) { \
      a[n][g_] = __builtin_amdgcn_raw_buffer_load_b128(rs, voff + g_ * 1024, so, 2); __builtin_amdgcn_sched_barrier(0); }
#define ISSUE_T(t) ISSUE((t) & 3, (((t) & 8) ? rv : rk), soff + ((t) & 7) * 16384 + ((t) >> 4) * 131072)
    if (wave == 0) STAMP_AT(0);
    const u32x4 xa = *(const u32x4*)(x + wave * D_ + lane * 8), xb = *(const u32x4*)(x + wave * D_ + 512 + lane * 8);
    wg_barrier();
    ISSUE_T(0) ISSUE_T(1)
    ln_row(xa, xb, sm + L_XN + wave * XN_STR, lane);
    if (wave == 0) STAMP_AT(13);
    wg_barrier();
    lds_wait_ge<0>(ctl + C_ST, 4);
    if (DIST > 2) { ISSUE_T(2) }
    if (DIST > 3) { ISSUE_T(3) }
    lds_add(ctl + C_K23);
    __builtin_amdgcn_s_setprio(S_PRIO);
    lds_wait_ge<0>(ctl + C_Q, 4);
    u32x4 qv;
    {
      float qs[8];
#pragma unroll
      for (int w = 0; w < 8; ++w) {
        const f32x4 p0 = *(LAS f32x4*)(sm + L_QP + w * 512 + c * 32), p1 = *(LAS f32x4*)(sm + L_QP + w * 512 + c * 32 + 16);
#pragma unroll
        for (int k = 0; k < 4; ++k) { qs[k] = w ? qs[k] + p0[k] : p0[k]; qs[4 + k] = w ? qs[4 + k] + p1[k] : p1[k]; }
      }
#pragma unroll
      for (int k = 0; k < 4; ++k) qv[k] = pack2(qs[2 * k], qs[2 * k + 1]);
    }
    const unsigned tag = (unsigned)lds_ld(ctl + C_TAG);
    if (wave == 0) STAMP_AT(1);
    float acc[8], m = -INFINITY, l = 0.f;
#pragma unroll
    for (int k = 0; k < 8; ++k) acc[k] = 0.f;
    const bool b3 = c & 8, b2 = c & 4, b1 = c & 2, b0 = c & 1;
    constexpr float SCALE = 0.08838834764831845f, LOG2E = 1.4426950408889634f;
    float p[2];
#define QK(n, x_) float x_[4]; _Pragma("unroll") for (int i = 0; i < 4; ++i) { float d = 0.f; \
      _Pragma("unroll") for (int k = 0; k < 4; ++k) d = dot2(a[n][i][k], qv[k], d); x_[i] = d; } \
      asm volatile("" : "+v"(x_[0]), "+v"(x_[1]), "+v"(x_[2]), "+v"(x_[3])); __builtin_amdgcn_sched_barrier(0);
    auto red4 = [&](const float (&x_)[4]) {
      const float y0 = (b3 ? x_[2] : x_[0]) + dpp<XOR8>(b3 ? x_[0] : x_[2]), y1 = (b3 ? x_[3] : x_[1]) + dpp<XOR8>(b3 ? x_[1] : x_[3]);
      return (b2 ? y1 : y0) + dpp<XOR7>(b2 ? y0 : y1);
    };
#define PV1(n, i, pr, bl) { const float pb = dpp<0x150 + (bl)>(pr); _Pragma("unroll") for (int k = 0; k < 4; ++k) { \
      acc[2 * k] = fmaf(pb, bflo(a[n][i][k]), acc[2 * k]); acc[2 * k + 1] = fmaf(pb, bfhi(a[n][i][k]), acc[2 * k + 1]); } }
#define PVG(n, pr, gl) PV1(n, 0, pr, (gl)) PV1(n, 1, pr, 4 + (gl)) PV1(n, 2, pr, 8 + (gl)) PV1(n, 3, pr, 12 + (gl)) \
      asm volatile("" : "+v"(acc[0]), "+v"(acc[1]), "+v"(acc[2]), "+v"(acc[3]), "+v"(acc[4]), "+v"(acc[5]), "+v"(acc[6]), "+v"(acc[7])); \
      __builtin_amdgcn_sched_barrier(0);
#define FSTAMP(i)
    auto block_head = [&]() {
      float z[4], s[2];
#define KSTEP(t) { QK((t) & 3, x_) ISSUE_T((t) + DIST) z[(t) & 3] = red4(x_); if (((t) & 3) == 3) { \
        const float u0 = (b1 ? z[2] : z[0]) + dpp<XOR2>(b1 ? z[0] : z[2]), u1 = (b1 ? z[3] : z[1]) + dpp<XOR2>(b1 ? z[1] : z[3]); \
        s[(t) >> 2] = (b0 ? u1 : u0) + dpp<XOR1>(b0 ? u0 : u1); } }
      KSTEP(0) KSTEP(1) KSTEP(2) KSTEP(3) FSTAMP(24) KSTEP(4) FSTAMP(25) KSTEP(5) KSTEP(6) KSTEP(7) FSTAMP(26)
      s[0] = rbf(rbf(s[0]) * SCALE) * LOG2E; s[1] = rbf(rbf(s[1]) * SCALE) * LOG2E;
      const float mn = fmaxf(m, wave_max(fmaxf(s[0], s[1])));
      const float alpha = __builtin_amdgcn_exp2f(m - mn);
      m = mn;
      p[0] = __builtin_amdgcn_exp2f(s[0] - mn); p[1] = __builtin_amdgcn_exp2f(s[1] - mn);
      l = fmaf(l, alpha, p[0] + p[1]);
#pragma unroll
      for (int k = 0; k < 8; ++k) acc[k] *= alpha;
    };
    auto finish = [&](int step, int row, int slot) {
#pragma unroll
      for (int k = 0; k < 8; ++k) acc[k] = xrow_sum(acc[k]);
      l = wave_sum(l);
      LAS char* dp = sm + L_DUMP + (step & 3) * 4 * DUMP_REC;
      const bool up = r & 1;
      *(LAS f32x4*)(dp + wave * DUMP_REC + 16 + c * 32 + (r & 1) * 16) = f32x4{up ? acc[4] : acc[0], up ? acc[5] : acc[1], up ? acc[6] : acc[2], up ? acc[7] : acc[3]};
      *(LAS f32x2*)(dp + wave * DUMP_REC) = f32x2{m, l};
      if ((lds_add(ctl + C_CNT + (step & 3)) & 3) == 3) {
        f32x2 ml[4]; f32x4 av[4];
#pragma unroll
        for (int w = 0; w < 4; ++w) { ml[w] = *(LAS f32x2*)(dp + w * DUMP_REC); av[w] = *(LAS f32x4*)(dp + w * DUMP_REC + 16 + (lane & 31) * 16); }
        const float M = fmaxf(fmaxf(ml[0][0], ml[1][0]), fmaxf(ml[2][0], ml[3][0]));
        float L = 0.f; f32x4 o = {0.f, 0.f, 0.f, 0.f};
#pragma unroll
        for (int w = 0; w < 4; ++w) { const float e = __builtin_amdgcn_exp2f(ml[w][0] - M); L = fmaf(e, ml[w][1], L); o += e * av[w]; }
        o *= 1.f / L;
        const float lse = M + __builtin_amdgcn_logf(L), lse_lo = lse - untag(tagw(lse, 0));
        if (lane >= 32) o = f32x4{lse, lse_lo, lse, lse_lo};
        st_tag4(rw, lane < 32 ? W_REC + (row * NSLOT + slot) * 512 + lane * 16 : lane == 32 ? W_LSE + row * 256 + slot * 16 : OOB, o, tag);
        lgkm0();
      }
    };
#define VSTEP(t) PVG((t) & 3, p[((t) >> 2) & 1], (t) & 3)
    for (int blk = 0; blk < NBLK - 1; ++blk) {
      block_head();
      VSTEP(8) ISSUE_T(8 + DIST) VSTEP(9) ISSUE_T(9 + DIST) VSTEP(10) ISSUE_T(10 + DIST) VSTEP(11) ISSUE_T(11 + DIST)
      FSTAMP(27) VSTEP(12) ISSUE_T(12 + DIST) VSTEP(13) ISSUE_T(13 + DIST) VSTEP(14) ISSUE_T(14 + DIST) VSTEP(15) ISSUE_T(15 + DIST)
      soff += 131072;
      if (wave == 0) { __hip_atomic_store(ctl + C_BLK, blk + 1, RLX_WG); STAMP_AT(16 + blk); }
    }
    auto ticket = [&](int j) {
      int t = -1;
      for (unsigned s = 0; (t = lds_ld(ctl + C_TICK + j)) < 0 && s < (SPIN_CAP << 2); ++s) __builtin_amdgcn_s_sleep(1);
      __hip_atomic_store(ctl + C_P + wave, j + 2, RLX_WG);
      cfence();
      return t < 0 ? NITEMS : t;
    };
    auto item_row = [&](int t) { return bat * 32 + 4 * (t / IPR) + (grp & 3); };
    auto item_off = [&](int t) { return (item_row(t & (NITEMS - 1)) << 20) + (NBLK * 512 + (t % IPR) * 128) * 256; };
    auto item_rs = [&](const char* base, int t) { return __builtin_amdgcn_make_buffer_rsrc((void*)base, 0, t < NITEMS ? 1 << 28 : 0, 0x00020000); };
#define ISSUE_I(u, ik_, iv_, io_) ISSUE(u, ((u) & 2) ? iv_ : ik_, (io_) + ((u) & 1) * 16384)
    int t, io;
    __amdgpu_buffer_rsrc_t ik = rk, iv = rv;
#define NEXT_TICKET(j) { t = ticket(j); io = item_off(t); ik = item_rs(Kc, t); iv = item_rs(Vc, t); }
#define VLAST(tt) if ((tt) + DIST == 16) { NEXT_TICKET(0) if (wave == 0) STAMP_AT(2); } \
      VSTEP(tt) if ((tt) + DIST < 16) { ISSUE_T((tt) + DIST) } else { ISSUE_I(((tt) + DIST) & 3, ik, iv, io) }
    block_head();
    __hip_atomic_store(ctl + C_P + wave, 1, RLX_WG);
    VLAST(8) VLAST(9) VLAST(10) VLAST(11) VLAST(12) VLAST(13) VLAST(14) VLAST(15)
    finish(0, bat * 32 + head, 0);
    for (int j = 1; t < NITEMS; ++j) {
      const int tc = t, ioc = io;
#define IPRE(u) if ((u) + DIST == 4) NEXT_TICKET(j)
#define IPOST(u) if ((u) + DIST < 4) { ISSUE_I((u) + DIST, rk, rv, ioc) } else { ISSUE_I(((u) + DIST) & 3, ik, iv, io) }
      qv = *(LAS u32x4*)(sm + L_QG + (tc / IPR) * 256 + c * 16);
      IPRE(0) QK(0, x0) IPOST(0)
      const float z0 = red4(x0);
      IPRE(1) QK(1, x1) IPOST(1)
      const float z1 = red4(x1);
      float s = (b1 ? z1 : z0) + dpp<XOR2>(b1 ? z0 : z1);
      s += dpp<XOR1>(s);
      s = rbf(rbf(s) * SCALE) * LOG2E;
      m = wave_max(s);
      const float pp = __builtin_amdgcn_exp2f(s - m);
      l = b0 ? 0.f : pp;
#pragma unroll
      for (int k = 0; k < 8; ++k) acc[k] = 0.f;
      IPRE(2) PVG(2, pp, 0) IPOST(2)
      PVG(3, pp, 2) IPOST(3)
      finish(j, item_row(tc), 1 + tc % IPR);
    }
    lds_add(ctl + C_SDONE);
    if (wave == 0) STAMP_AT(3);
    return;
  }

  const int hw = wid < 8 ? wid : wid - 8;
  u64 tag64 = 0;
  unsigned tag;
  if (hw < 4) {
    __builtin_amdgcn_s_setprio(3);
    int vzero;
    asm volatile("v_mov_b32 %0, 0" : "=v"(vzero));
    u64 epoch_v = __hip_atomic_load((GAS u64*)(ws + W_EPOCH + vzero), RLX_AGENT);
    __builtin_amdgcn_sched_barrier(0);
    const __amdgpu_buffer_rsrc_t rq = __builtin_amdgcn_make_buffer_rsrc((void*)wq, 0, D_ * HD * 2, 0x00020000);
    u32x4 w[8];
#pragma unroll
    for (int i = 0; i < 8; ++i) w[i] = __builtin_amdgcn_raw_buffer_load_b128(rq, r * 8192 + c * 16, (128 * bat + 32 * hw + 4 * i) * 8192 + head * 256, WQ_AUX);
    wg_barrier();
    asm volatile("" : "+v"(epoch_v));
#pragma unroll
    for (int i = 0; i < 8; ++i) *(LAS u32x4*)(sm + L_W + (c >> 1) * W_TS + rowpos(32 * hw + 4 * i + r) * 32 + (c & 1) * 16) = w[i];
    tag64 = (((u64)(unsigned)__builtin_amdgcn_readfirstlane((int)(epoch_v >> 32)) << 32) | (unsigned)__builtin_amdgcn_readfirstlane((int)epoch_v)) + 1;
    asm volatile("" :: "s"(tag64));
    tag = (unsigned)tag64 & 0xffu;
    if (hw == 0) STAMP_AT(12);
    wg_barrier();
    if (hw == 0) { ctl[C_TAG] = (int)tag; STAMP_AT(10); }
    f32x4 d0 = {0.f, 0.f, 0.f, 0.f}, d1 = d0;
#pragma unroll
    for (int s = 0; s < 4; ++s) {
      const bf16x8 xf = *(LAS bf16x8*)(sm + L_XN + (lane & 7) * XN_STR + (128 * bat + 32 * s + 8 * r) * 2);
      d0 = __builtin_amdgcn_mfma_f32_16x16x32_bf16(ld_bfrag(sm + L_W + (2 * hw) * W_TS, 32 * s, lane), xf, d0, 0, 0, 0);
      d1 = __builtin_amdgcn_mfma_f32_16x16x32_bf16(ld_bfrag(sm + L_W + (2 * hw + 1) * W_TS, 32 * s, lane), xf, d1, 0, 0, 0);
    }
    st_tag4(rw, W_QS + (((c & 7) * 32 + head) * 8 + bat) * 512 + (32 * hw + 2 * (c & 8) + 4 * r) * 4, (c & 8) ? d1 : d0, tag);
    lds_add(ctl + C_ST);
    if (hw == 0) STAMP_AT(11);
    lds_wait_ge<0>(ctl + C_K23, 4);
    if (hw == 0) STAMP_AT(22);
    u32x4 g;
    for (unsigned s = 0; s < SPIN_CAP; ++s) {
      g = ld_tag4(rw, W_QS + (bat * 32 + head) * 4096 + hw * 1024 + lane * 16);
      if (s == 0) lds_add(ctl + C_GI);
      cfence();
      if (hw == 0) STAMP_VAL(14, s + 1);
      if (__all(tag_ok(g, tag))) break;
    }
    if (hw == 0) STAMP_AT(23);
    *(LAS f32x4*)(sm + L_QP + hw * 1024 + lane * 16) = untag4(g);
    lds_add(ctl + C_Q);
    if (hw == 0) {
      lds_wait_ge<0>(ctl + C_Q, 4);
      f32x4 q = *(LAS f32x4*)(sm + L_QP + (lane & 31) * 16);
#pragma unroll
      for (int i = 1; i < 8; ++i) q += *(LAS f32x4*)(sm + L_QP + i * 512 + (lane & 31) * 16);
      st_tag4(rw, lane < 32 ? W_QG + (bat * 32 + head) * 512 + lane * 16 : OOB, f32x4{rbf(q[0]), rbf(q[1]), rbf(q[2]), rbf(q[3])}, tag);
    } else {
      __builtin_amdgcn_s_setprio(H_PRIO);
    }
  } else {
    const int gw = hw - 4;
    __builtin_amdgcn_s_setprio(H_PRIO);
    {
      const u32x4 xa = *(const u32x4*)(x + hw * D_ + lane * 8), xb = *(const u32x4*)(x + hw * D_ + 512 + lane * 8);
      wg_barrier();
      if (gw < 3) ctl[gw * 64 + lane] = gw ? -1 : 0;
      ln_row(xa, xb, sm + L_XN + hw * XN_STR, lane);
    }
    wg_barrier();
    if (xcd != (grp & 7)) {
      const int fb = grp * 7 + xcd - (xcd > (grp & 7));
      const int cb = fb >> 2, ks = fb & 3, row0 = 256 * ks + 64 * gw;
      const char* wp = ((lane & 8) ? wu : wg) + (size_t)(row0 + 8 * r) * (F_ * 2) + cb * 128 + (lane & 7) * 16;
      lds_wait_ge<1>(ctl + C_GI, 4);
      u32x4 w[16];
#pragma unroll
      for (int i = 0; i < 16; ++i) { w[i] = __builtin_nontemporal_load((const u32x4*)(wp + (32 * (i >> 3) + (i & 7)) * (F_ * 2))); __builtin_amdgcn_sched_barrier(0); }
      tag = (unsigned)lds_ld(ctl + C_TAG);
      f32x4 d[8];
#pragma unroll
      for (int j = 0; j < 8; ++j) d[j] = f32x4{0.f, 0.f, 0.f, 0.f};
#pragma unroll
      for (int s = 0; s < 2; ++s) {
        const bf16x8 af = *(LAS bf16x8*)(sm + L_XN + (lane & 7) * XN_STR + (row0 + 32 * s + 8 * r) * 2);
#pragma unroll
        for (int j = 0; j < 8; ++j) {
          u32x4 bq;
#pragma unroll
          for (int mm = 0; mm < 4; ++mm) bq[mm] = __builtin_amdgcn_perm(w[8 * s + 2 * mm + 1][j >> 1], w[8 * s + 2 * mm][j >> 1], (j & 1) ? 0x07060302 : 0x05040100);
          d[j] = __builtin_amdgcn_mfma_f32_16x16x32_bf16(af, __builtin_bit_cast(bf16x8, bq), d[j], 0, 0, 0);
        }
      }
      {
        const int b0_ = 4 * (r & 1) + 2 * (r >> 1);
        const bool hi = r >> 1;
        LAS char* dst = sm + L_GUP + gw * 4096 + b0_ * 512 + c * 32;
#pragma unroll
        for (int e = 0; e < 2; ++e) {
          *(LAS f32x4*)(dst + e * 512) = f32x4{hi ? d[0][2 + e] : d[0][e], hi ? d[1][2 + e] : d[1][e], hi ? d[2][2 + e] : d[2][e], hi ? d[3][2 + e] : d[3][e]};
          *(LAS f32x4*)(dst + e * 512 + 16) = f32x4{hi ? d[4][2 + e] : d[4][e], hi ? d[5][2 + e] : d[5][e], hi ? d[6][2 + e] : d[6][e], hi ? d[7][2 + e] : d[7][e]};
        }
      }
      lds_add(ctl + C_GU);
      lds_wait_ge<1>(ctl + C_GU, 4);
      {
        LAS char* src = sm + L_GUP + ((lane >> 2) & 7) * 512 + (lane >> 5) * 256 + gw * 64 + (lane & 3) * 16;
        const f32x4 sum = (*(LAS f32x4*)src + *(LAS f32x4*)(src + 4096)) + (*(LAS f32x4*)(src + 8192) + *(LAS f32x4*)(src + 12288));
        st_tag4(rw, W_GS + ((4 * cb + gw) * 4 + ks) * 1024 + lane * 16, sum, tag);
      }
      if (gw == 3) STAMP_AT(28);
      if (gw == 1) {
        lds_wait_ge<32>(ctl + C_BLK, WAIT_G2);
        nap((blockIdx.x * 5) & (STAGGER - 1));
        u32x4 g[4];
        for (unsigned s = 0; s < SPIN_CAP; ++s) {
#pragma unroll
          for (int i = 0; i < 4; ++i) g[i] = ld_tag4(rw, W_GS + fb * 4096 + i * 1024 + lane * 16);
          cfence();
          if (__all(tag_ok(g[0], tag) && tag_ok(g[1], tag) && tag_ok(g[2], tag) && tag_ok(g[3], tag))) break;
          __builtin_amdgcn_s_sleep(32);
        }
        const f32x4 sv = (untag4(g[0]) + untag4(g[1])) + (untag4(g[2]) + untag4(g[3]));
        f32x4 h;
#pragma unroll
        for (int k = 0; k < 4; ++k) {
          auto t = __builtin_amdgcn_permlane32_swap(__float_as_uint(sv[k]), __float_as_uint(sv[k]), false, false);
          h[k] = rbf(rbf(__uint_as_float(t[0])) * rbf(__uint_as_float(t[1])));
        }
        st_tag4(rw, lane < 32 ? W_HG + (fb / 7) * 4096 + (lane >> 2) * 512 + (fb % 7) * 64 + (lane & 3) * 16 : OOB, h, tag);
        STAMP_AT(4);
      }
    } else {
      lds_wait_ge<1>(ctl + C_GI, 4);
      tag = (unsigned)lds_ld(ctl + C_TAG);
    }
  }
  GAS u64* const cnt = (GAS u64*)(ws + W_CNT + grp * 256);
  if (hw == 0) {
    lds_wait_ge<8>(ctl + C_QG, 1);
    for (int got = 0;; ++got) {
      for (unsigned s = 0; s < (SPIN_CAP << 2); ++s) {
        const int p0 = lds_ld(ctl + C_P), p1 = lds_ld(ctl + C_P + 1), p2 = lds_ld(ctl + C_P + 2), p3 = lds_ld(ctl + C_P + 3);
        if (max(max(p0, p1), max(p2, p3)) > got && min(min(p0, p1), min(p2, p3)) >= got - SKEW) break;
        __builtin_amdgcn_s_sleep(PROG_SLEEP);
      }
      int t = 0;
      if (lane == 0) t = (int)(__hip_atomic_fetch_add(cnt, 1ull, RLX_AGENT) & 0xffff);
      t = __builtin_amdgcn_readfirstlane(t);
      if (lane == 0) __hip_atomic_store(ctl + C_TICK + got, t, RLX_WG);
      if (t >= NITEMS - W_EARLY && lane == 0) __hip_atomic_store(ctl + C_END, 1, RLX_WG);
      if (t >= NITEMS || got >= NITEMS) break;
    }
    lds_add(ctl + C_WC);
    STAMP_AT(5);
  } else {
    if (hw == 3) {
      const u64 fresh = tag64 << 16;
      lds_wait_ge<32>(ctl + C_BLK, WAIT_Q);
      nap((blockIdx.x * 11) & (STAGGER - 1));
      if (lane == 0) {
        u64 v = __hip_atomic_load(cnt, RLX_AGENT);
        if ((v >> 16) != (fresh >> 16)) __hip_atomic_compare_exchange_strong(cnt, &v, fresh, __ATOMIC_RELAXED, RLX_AGENT);
        asm volatile("" :: "v"(v));
      }
      asm volatile("s_waitcnt vmcnt(0)" ::: "memory");
      for (unsigned s = 0; s < SPIN_CAP; ++s) {
        u32x4 g[4];
#pragma unroll
        for (int i = 0; i < 4; ++i) g[i] = ld_tag4(rw, W_QG + (bat * 32 + 8 * i + 4 * (lane >> 5) + (grp & 3)) * 512 + (lane & 31) * 16);
        cfence();
#pragma unroll
        for (int i = 0; i < 4; ++i) *(LAS u32x2*)(sm + L_QG + i * 512 + lane * 8) = hi16x4(g[i]);
        if (__all(tag_ok(g[0], tag) && tag_ok(g[1], tag) && tag_ok(g[2], tag) && tag_ok(g[3], tag))) break;
        __builtin_amdgcn_s_sleep(32);
      }
      lds_add(ctl + C_QG);
    }
    if (hw == 2) {
      lds_wait_ge<32>(ctl + C_BLK, WAIT_H);
      nap((blockIdx.x * 3) & (STAGGER - 1));
      for (unsigned s = 0; s < SPIN_CAP; ++s) {
        u32x4 g[4];
        const bool live = (lane & 31) < 28;
#pragma unroll
        for (int i = 0; i < 4; ++i) g[i] = ld_tag4(rw, live ? W_HG + head * 4096 + i * 1024 + lane * 16 : OOB);
        cfence();
#pragma unroll
        for (int i = 0; i < 4; ++i) *(LAS u32x2*)(sm + L_HA + (2 * i + (lane >> 5)) * A_STR + (lane & 31) * 8) = hi16x4(g[i]);
        if (__all(!live || (tag_ok(g[0], tag) && tag_ok(g[1], tag) && tag_ok(g[2], tag) && tag_ok(g[3], tag)))) break;
        __builtin_amdgcn_s_sleep(64);
      }
    }
    if (hw >= 4) {
      lds_wait_ge<END_SLEEP>(ctl + C_END, 1);
      const __amdgpu_buffer_rsrc_t ro = __builtin_amdgcn_make_buffer_rsrc((void*)wo, 0, HD * D_ * 2, 0x00020000);
      const __amdgpu_buffer_rsrc_t rd = __builtin_amdgcn_make_buffer_rsrc((void*)wd, 0, F_ * D_ * 2, 0x00020000);
      u32x4 wv[15];
#pragma unroll
      for (int i = 0; i < 15; ++i)
        wv[i] = __builtin_amdgcn_raw_buffer_load_b128(i < 8 ? ro : rd, r * 2048 + c * 16, ((i < 8 ? 128 * head : 112 * head - 128) + 4 * (hw - 4) + 16 * i) * 2048 + bat * 256, W_AUX);
#pragma unroll
      for (int i = 0; i < 15; ++i) *(LAS u32x4*)(sm + L_W + (c >> 1) * W_TS + rowpos(4 * (hw - 4) + 16 * i + r) * 32 + (c & 1) * 16) = wv[i];
    }
    lds_add(ctl + C_WC);
  }
  f32x4 dacc = {0.f, 0.f, 0.f, 0.f};
  bf16x8 bo[4];
  {
    lds_wait_ge<4>(ctl + C_WC, 8);
    LAS char* img = sm + L_W + hw * W_TS;
#pragma unroll
    for (int s = 0; s < 4; ++s) bo[s] = ld_bfrag(img, 32 * s, lane);
#pragma unroll
    for (int s = 0; s < 3; ++s)
      dacc = __builtin_amdgcn_mfma_f32_16x16x32_bf16(ld_bfrag(img, 128 + 32 * s, lane), *(LAS bf16x8*)(sm + L_HA + (lane & 7) * A_STR + (32 * s + 8 * r) * 2), dacc, 0, 0, 0);
    dacc = __builtin_amdgcn_mfma_f32_16x16x16bf16_1k(ld_tr(img + rowpos(224 + 4 * r + ((lane >> 2) & 3)) * 32 + (lane & 3) * 8),
                                                     *(LAS s16x4*)(sm + L_HA + (lane & 7) * A_STR + (96 + 4 * r) * 2), dacc, 0, 0, 0);
  }
  const int rchunk = blockIdx.x >> 3, rb = blockIdx.x & 7;
  u32x2 xres = {0u, 0u};
  if (hw == 0) xres = *(const u32x2*)(x + rb * D_ + rchunk * 32 + (lane & 7) * 4);
  if (hw == 0) STAMP_AT(6);
  lds_wait_ge<1>(ctl + C_SDONE, 4);
  if (hw == 0) STAMP_AT(7);
  {
    const int row = hw * 32 + head;
    constexpr int NL = (NSLOT + 1) / 2;
    u32x4 g[NL], e;
    const bool odd = lane >> 5;
    for (unsigned s = 0; s < SPIN_CAP; ++s) {
      e = ld_tag4(rw, W_LSE + row * 256 + (lane < NSLOT ? lane : 0) * 16);
#pragma unroll
      for (int i = 0; i < NL; ++i) g[i] = ld_tag4(rw, (2 * i + 1 < NSLOT || !odd) ? W_REC + row * NSLOT * 512 + i * 1024 + lane * 16 : OOB);
      cfence();
      bool ok = tag_ok(e, tag);
#pragma unroll
      for (int i = 0; i < NL; ++i) ok &= tag_ok(g[i], tag) || (2 * i + 1 >= NSLOT && odd);
      if (__all(ok)) break;
      __builtin_amdgcn_s_sleep(2);
    }
    const float lse_l = untag(e[0]) + untag(e[1]);
    float lse[NSLOT], M = -INFINITY;
#pragma unroll
    for (int i = 0; i < NSLOT; ++i) { lse[i] = __uint_as_float(__builtin_amdgcn_readlane(__float_as_uint(lse_l), i)); M = fmaxf(M, lse[i]); }
    float wgt[NSLOT + 1], L = 0.f;
#pragma unroll
    for (int i = 0; i < NSLOT; ++i) { wgt[i] = __builtin_amdgcn_exp2f(lse[i] - M); L += wgt[i]; }
    wgt[NSLOT] = 0.f;
    f32x4 o = {0.f, 0.f, 0.f, 0.f};
#pragma unroll
    for (int i = 0; i < NL; ++i) o += (odd ? wgt[2 * i + 1] : wgt[2 * i]) * untag4(g[i]);
    const float inv = 1.f / L;
#pragma unroll
    for (int k = 0; k < 4; ++k) o[k] = x32_sum(o[k]) * inv;
    *(LAS u32x2*)(sm + L_OA + hw * A_STR + (lane & 31) * 8) = u32x2{pack2(o[0], o[1]), pack2(o[2], o[3])};
  }
  lds_add(ctl + C_OA);
  lds_wait_ge<0>(ctl + C_OA, 8);
  if (hw == 0) STAMP_AT(8);
#pragma unroll
  for (int s = 0; s < 4; ++s)
    dacc = __builtin_amdgcn_mfma_f32_16x16x32_bf16(bo[s], *(LAS bf16x8*)(sm + L_OA + (lane & 7) * A_STR + (32 * s + 8 * r) * 2), dacc, 0, 0, 0);
  st_tag4(rw, (c & 8) ? OOB : W_PART + (((4 * bat + (hw >> 1)) * 8 + (c & 7)) * 32 + head) * 128 + (16 * (hw & 1) + 4 * r) * 4, dacc, tag);
  if (hw != 0) return;
  {
    u32x4 g[4];
    for (unsigned s = 0; s < SPIN_CAP; ++s) {
#pragma unroll
      for (int k = 0; k < 4; ++k) g[k] = ld_tag4(rw, W_PART + (rchunk * 8 + rb) * 4096 + k * 1024 + lane * 16);
      cfence();
      if (__all(tag_ok(g[0], tag) && tag_ok(g[1], tag) && tag_ok(g[2], tag) && tag_ok(g[3], tag))) break;
      __builtin_amdgcn_s_sleep(1);
    }
    f32x4 sum = (untag4(g[0]) + untag4(g[1])) + (untag4(g[2]) + untag4(g[3]));
#pragma unroll
    for (int k = 0; k < 4; ++k) { float t = sum[k]; t += dpp<XOR8>(t); sum[k] = xrow_sum(t); }
    if (lane < 8)
      *(u32x2*)(out + rb * D_ + rchunk * 32 + lane * 4) = u32x2{pack2(bflo(xres[0]) + sum[0], bfhi(xres[0]) + sum[1]), pack2(bflo(xres[1]) + sum[2], bfhi(xres[1]) + sum[3])};
    if (blockIdx.x == 0 && lane == 0) __hip_atomic_store((GAS u64*)(ws + W_EPOCH), tag64, RLX_AGENT);
    STAMP_AT(9);
    STAMP_DUMP();
  }
}

extern "C" void kernel_launch(void* const* d_in, const int* in_sizes, int n_in, void* d_out, int out_size, void* d_ws, size_t ws_size,
                              hipStream_t stream) {
  fused_decode<<<256, NTHR, 0, stream>>>((const unsigned short*)d_in[0], (const char*)d_in[1], (const char*)d_in[2], (const char*)d_in[3],
                                         (const char*)d_in[4], (const char*)d_in[6], (const char*)d_in[7], (const char*)d_in[8],
                                         (unsigned short*)d_out, (char*)d_ws);
}
```

```cpp
#include <hip/hip_runtime.h>
#include <hip/hip_bf16.h>
#include <stdint.h>
#include <type_traits>

typedef unsigned u32x4 __attribute__((ext_vector_type(4)));
typedef unsigned u32x2 __attribute__((ext_vector_type(2)));
typedef float f32x4 __attribute__((ext_vector_type(4)));
typedef float f32x2 __attribute__((ext_vector_type(2)));
typedef short s16x4 __attribute__((ext_vector_type(4)));
typedef short s16x8 __attribute__((ext_vector_type(8)));
typedef __bf16 bf16x2 __attribute__((ext_vector_type(2)));
typedef __bf16 bf16x8 __attribute__((ext_vector_type(8)));
typedef unsigned long long u64;
#define GAS __attribute__((address_space(1)))
#define LAS __attribute__((address_space(3)))
#define RLX_AGENT __ATOMIC_RELAXED, __HIP_MEMORY_SCOPE_AGENT
#define RLX_WG __ATOMIC_RELAXED, __HIP_MEMORY_SCOPE_WORKGROUP

constexpr int D_ = 1024, SK = 4096, DH = 128, F_ = 3584, HD = 4096;
constexpr int NTHR = 768;
#define NDU_ 4
constexpr int NDU = NDU_, NSU = 16 - NDU;
constexpr int NITEMS = 8 * NDU;
constexpr int NSLOT = 1 + NDU;
constexpr unsigned SPIN_CAP = 1u << 17;
#define WQ_AUX 2
#define W_AUX 2
#define GU_AUX 2
#define S_PRIO 0
#define H_PRIO 2
#define END_SLEEP 8
#define PROG_SLEEP 2
#define WAIT_G2 6
#define WAIT_Q 10
#define WAIT_H 16
#define STAGGER 16
#define LOOK2 0
#define WS_OFF 0
#define SKEW -1
#define DIST 3
#define W_BLK 20

constexpr int L_XN = 0, XN_STR = 2064;
constexpr int L_QP = L_XN + 8 * XN_STR;
constexpr int L_GUP = L_QP + 4096;
constexpr int L_SC = L_GUP + 16384;
constexpr int L_DUMP = L_SC + 16384, DUMP_REC = 528;
constexpr int L_W = L_DUMP + 16 * DUMP_REC, W_TS = 8224;
constexpr int L_OA = L_W + 8 * W_TS, A_STR = 272;
constexpr int L_HA = L_OA + 8 * A_STR;
constexpr int L_CTL = L_HA + 8 * A_STR;
constexpr int C_TICK = 64, C_MX = 36, C_M = 40, C_P = 44, C_QG = 48, C_CNT = 49, C_GU = 53, C_WC = 54, C_OA = 55, C_END = 56, C_SDONE = 57, C_TAG = 58, C_ST = 59, C_K23 = 60, C_GI = 61, C_BLK = 62, C_Q = 63, C_WORDS = 64 + 128;
constexpr int L_BYTES = L_CTL + C_WORDS * 4;
constexpr unsigned W_EPOCH = 0, W_CNT = 4096, W_P = 16384, W_HG = W_P + 256 * NDU * 1024, W_L = W_HG + 32 * 4096, W_REC = W_L + 256 * 128,
                   W_PART = W_REC + 256 * NSLOT * 512, W_QS = W_PART + 256 * 4096, W_GS = W_QS + 256 * 4096, W_END = W_GS + 224 * 4096, W_STAMP = 8u << 20;
static_assert(W_END <= W_STAMP, "workspace map");
constexpr int OOB = 0x7ffffff0;

__device__ __forceinline__ float bflo(unsigned x) { return __builtin_bit_cast(float, x << 16); }
__device__ __forceinline__ float bfhi(unsigned x) { return __builtin_bit_cast(float, x & 0xffff0000u); }
__device__ __forceinline__ float rbf(float x) { return (float)(__bf16)x; }
__device__ __forceinline__ unsigned pack2(float lo, float hi) { bf16x2 v = {(__bf16)lo, (__bf16)hi}; return __builtin_bit_cast(unsigned, v); }
__device__ __forceinline__ float dot2(unsigned k, unsigned q, float acc) {
  return __builtin_amdgcn_fdot2_f32_bf16(__builtin_bit_cast(bf16x2, k), __builtin_bit_cast(bf16x2, q), acc, false);
}
template <int CTRL> __device__ __forceinline__ float dpp(float x) {
  return __builtin_bit_cast(float, __builtin_amdgcn_mov_dpp(__builtin_bit_cast(int, x), CTRL, 0xf, 0xf, true));
}
constexpr int XOR1 = 0xB1, XOR2 = 0x4E, XOR7 = 0x141, XOR8 = 0x128;
__device__ __forceinline__ float xrow_max(float x) {
  auto s = __builtin_amdgcn_permlane16_swap(__float_as_uint(x), __float_as_uint(x), false, false);
  x = fmaxf(__uint_as_float(s[0]), __uint_as_float(s[1]));
  auto t = __builtin_amdgcn_permlane32_swap(__float_as_uint(x), __float_as_uint(x), false, false);
  return fmaxf(__uint_as_float(t[0]), __uint_as_float(t[1]));
}
__device__ __forceinline__ float x32_sum(float x) {
  auto t = __builtin_amdgcn_permlane32_swap(__float_as_uint(x), __float_as_uint(x), false, false);
  return __uint_as_float(t[0]) + __uint_as_float(t[1]);
}
__device__ __forceinline__ float xrow_sum(float x) {
  auto s = __builtin_amdgcn_permlane16_swap(__float_as_uint(x), __float_as_uint(x), false, false);
  return x32_sum(__uint_as_float(s[0]) + __uint_as_float(s[1]));
}
__device__ __forceinline__ float wave_sum(float x) {
  x += dpp<XOR1>(x); x += dpp<XOR2>(x); x += dpp<XOR7>(x); x += dpp<XOR8>(x); return xrow_sum(x);
}
__device__ __forceinline__ float wave_max(float x) {
  x = fmaxf(x, dpp<XOR1>(x)); x = fmaxf(x, dpp<XOR2>(x)); x = fmaxf(x, dpp<XOR7>(x)); x = fmaxf(x, dpp<XOR8>(x)); return xrow_max(x);
}
__device__ __forceinline__ void lgkm0() { asm volatile("s_waitcnt lgkmcnt(0)" ::: "memory"); }
__device__ __forceinline__ void cfence() { asm volatile("" ::: "memory"); }
__device__ __forceinline__ void wg_barrier() { asm volatile("s_waitcnt lgkmcnt(0)\n\ts_barrier" ::: "memory"); }
__device__ __forceinline__ int lds_ld(LAS int* p) { return __builtin_amdgcn_readfirstlane(__hip_atomic_load(p, RLX_WG)); }
__device__ __forceinline__ int lds_add(LAS int* p) {
  int old; u64 keep;
  asm volatile("s_waitcnt lgkmcnt(0)\n\ts_mov_b64 %1, exec\n\ts_mov_b64 exec, 1\n\tds_add_rtn_u32 %0, %2, %3\n\ts_mov_b64 exec, %1\n\ts_waitcnt lgkmcnt(0)"
               : "=&v"(old), "=&s"(keep) : "v"(p), "v"(1) : "memory");
  return __builtin_amdgcn_readfirstlane(old);
}
template <int SLEEP> __device__ __forceinline__ void lds_wait_ge(LAS int* p, int want) {
  for (unsigned s = 0; lds_ld(p) < want && s < (SPIN_CAP << 4); ++s) __builtin_amdgcn_s_sleep(SLEEP);
  cfence();
}
__device__ __forceinline__ unsigned tagw(float v, unsigned tag) { return ((__float_as_uint(v) + 0x80u) & 0xffffff00u) | tag; }
__device__ __forceinline__ float untag(unsigned w) { return __uint_as_float(w & 0xffffff00u); }
__device__ __forceinline__ void st_tag4(__amdgpu_buffer_rsrc_t rw, int off, f32x4 v, unsigned tag) {
  __builtin_amdgcn_raw_buffer_store_b128(u32x4{tagw(v[0], tag), tagw(v[1], tag), tagw(v[2], tag), tagw(v[3], tag)}, rw, off, 0, 16);
}
__device__ __forceinline__ u32x4 ld_tag4(__amdgpu_buffer_rsrc_t rw, int off) { return __builtin_amdgcn_raw_buffer_load_b128(rw, off, 0, 16); }
__device__ __forceinline__ bool tag_ok(u32x4 w, unsigned tag) { return (((w[0] ^ tag) | (w[1] ^ tag) | (w[2] ^ tag) | (w[3] ^ tag)) & 0xffu) == 0u; }
__device__ __forceinline__ f32x4 untag4(u32x4 w) { return f32x4{untag(w[0]), untag(w[1]), untag(w[2]), untag(w[3])}; }
__device__ __forceinline__ u32x2 hi16x4(u32x4 w) { return u32x2{__builtin_amdgcn_perm(w[1], w[0], 0x07060302), __builtin_amdgcn_perm(w[3], w[2], 0x07060302)}; }
__device__ __forceinline__ void nap(int n) { for (int i = 0; i < n; ++i) __builtin_amdgcn_s_sleep(16); }
__device__ __forceinline__ int rowpos(int row) { return row ^ ((row >> 1) & 4); }
__device__ __forceinline__ s16x4 ld_tr(LAS char* p) { return __builtin_amdgcn_ds_read_tr16_b64_v4i16((LAS s16x4*)p); }
__device__ __forceinline__ bf16x8 ld_bfrag(LAS char* img, int row0, int lane) {
  const int g = lane >> 4, q = (lane >> 2) & 3, p = lane & 3;
  const s16x4 lo = ld_tr(img + rowpos(row0 + 8 * g + q) * 32 + p * 8), hi = ld_tr(img + rowpos(row0 + 8 * g + 4 + q) * 32 + p * 8);
  return __builtin_bit_cast(bf16x8, __builtin_shufflevector(lo, hi, 0, 1, 2, 3, 4, 5, 6, 7));
}
#define STAMP_AT(i) do { } while (0)
#define STAMP_VAL(i, v) do { } while (0)
#define STAMP_DUMP() do { } while (0)
#define L_EXTRA 0

__device__ __forceinline__ void ln_row(const u32x4 xa, const u32x4 xb, LAS char* dst, int lane) {
  float v[16];
#pragma unroll
  for (int j = 0; j < 4; ++j) { v[2 * j] = bflo(xa[j]); v[2 * j + 1] = bfhi(xa[j]); v[8 + 2 * j] = bflo(xb[j]); v[8 + 2 * j + 1] = bfhi(xb[j]); }
  float s = 0.f;
#pragma unroll
  for (int j = 0; j < 16; ++j) s += v[j];
  const float mean = wave_sum(s) * (1.f / D_);
  float q = 0.f;
#pragma unroll
  for (int j = 0; j < 16; ++j) { const float d = v[j] - mean; q = fmaf(d, d, q); }
  const float var = wave_sum(q) * (1.f / D_);
  const float mb = rbf(mean), rs = 1.f / rbf(sqrtf(rbf(rbf(var) + 1e-5f)));
  u32x4 o0, o1;
#pragma unroll
  for (int j = 0; j < 4; ++j) {
    o0[j] = pack2(rbf(v[2 * j] - mb) * rs, rbf(v[2 * j + 1] - mb) * rs);
    o1[j] = pack2(rbf(v[8 + 2 * j] - mb) * rs, rbf(v[8 + 2 * j + 1] - mb) * rs);
  }
  *(LAS u32x4*)(dst + lane * 16) = o0;
  *(LAS u32x4*)(dst + 1024 + lane * 16) = o1;
}

__global__ void __launch_bounds__(NTHR, 3)
fused_decode(const unsigned short* __restrict__ x, const char* __restrict__ Kc, const char* __restrict__ Vc, const char* __restrict__ wq,
             const char* __restrict__ wo, const char* __restrict__ wg, const char* __restrict__ wu, const char* __restrict__ wd,
             unsigned short* __restrict__ out, char* ws) {
  __shared__ __attribute__((aligned(16))) char smem_[L_BYTES + L_EXTRA];
  LAS char* const sm = (LAS char*)smem_;
  LAS int* const ctl = (LAS int*)(sm + L_CTL);
  const int tid = threadIdx.x, lane = tid & 63, wid = __builtin_amdgcn_readfirstlane(tid >> 6), r = lane >> 4, c = lane & 15;
  const int xcd = blockIdx.x & 7, grp = blockIdx.x >> 3;
  const int head = 4 * xcd + (grp & 3), bat = grp >> 2;

  const __amdgpu_buffer_rsrc_t rw = __builtin_amdgcn_make_buffer_rsrc((void*)ws, 0, W_END, 0x00020000);

  if (wid < 4) {
    const int wave = wid;
    const __amdgpu_buffer_rsrc_t rk = __builtin_amdgcn_make_buffer_rsrc((void*)Kc, 0, 1 << 28, 0x00020000);
    const __amdgpu_buffer_rsrc_t rv = __builtin_amdgcn_make_buffer_rsrc((void*)Vc, 0, 1 << 28, 0x00020000);
    const int voff = wave * 4096 + lane * 16;
    const int row0 = bat * 32 + head;
    u32x4 a[4][4];
#define ISSUE(n, rs, so) _Pragma("unroll") for (int g_ = 0; g_ < 4; ++g_) { \
      a[n][g_] = __builtin_amdgcn_raw_buffer_load_b128(rs, voff + g_ * 1024, so, 2); __builtin_amdgcn_sched_barrier(0); }
#define AHEAD(t) if ((t) + DIST < 4) { ISSUE((t) + DIST, rc, oc + ((t) + DIST) * 16384) } else { ISSUE(((t) + DIST) & 3, rn, on + (((t) + DIST) & 3) * 16384) }
    if (wave == 0) STAMP_AT(0);
    int oc = row0 << 20;
    {
      const u32x4 xa = *(const u32x4*)(x + wave * D_ + lane * 8), xb = *(const u32x4*)(x + wave * D_ + 512 + lane * 8);
      wg_barrier();
      ISSUE(0, rk, oc) ISSUE(1, rk, oc + 16384)
      ln_row(xa, xb, sm + L_XN + wave * XN_STR, lane);
    }
    if (wave == 0) STAMP_AT(13);
    wg_barrier();
    lds_wait_ge<0>(ctl + C_ST, 4);
    if (DIST > 2) { ISSUE(2, rk, oc + 32768) }
    if (DIST > 3) { ISSUE(3, rk, oc + 49152) }
    lds_add(ctl + C_K23);
    __builtin_amdgcn_s_setprio(S_PRIO);
    lds_wait_ge<0>(ctl + C_Q, 4);
    u32x4 qv;
    {
      float qs[8];
#pragma unroll
      for (int w = 0; w < 8; ++w) {
        const f32x4 p0 = *(LAS f32x4*)(sm + L_QP + w * 512 + c * 32), p1 = *(LAS f32x4*)(sm + L_QP + w * 512 + c * 32 + 16);
#pragma unroll
        for (int k = 0; k < 4; ++k) { qs[k] = w ? qs[k] + p0[k] : p0[k]; qs[4 + k] = w ? qs[4 + k] + p1[k] : p1[k]; }
      }
#pragma unroll
      for (int k = 0; k < 4; ++k) qv[k] = pack2(qs[2 * k], qs[2 * k + 1]);
    }
    const unsigned tag = (unsigned)lds_ld(ctl + C_TAG);
    if (wave == 0) STAMP_AT(1);
    const bool b3 = c & 8, b2 = c & 4, b1 = c & 2, b0 = c & 1;
    constexpr float SCALE = 0.08838834764831845f, LOG2E = 1.4426950408889634f;
    LAS float* const sc = (LAS float*)(sm + L_SC) + wave * 1024 + lane;
    float mx = -INFINITY;
    for (int u = 0; u < 16; ++u) {
      const __amdgpu_buffer_rsrc_t rc = rk, rn = __builtin_amdgcn_make_buffer_rsrc((void*)(u < 15 ? Kc : Vc), 0, 1 << 28, 0x00020000);
      const int on = u < 15 ? oc + 65536 : row0 << 20;
      float z[4];
#define KSTEP(t) { float x_[4]; _Pragma("unroll") for (int i = 0; i < 4; ++i) { float d = 0.f; \
        _Pragma("unroll") for (int k = 0; k < 4; ++k) d = dot2(a[t][i][k], qv[k], d); x_[i] = d; } \
        asm volatile("" : "+v"(x_[0]), "+v"(x_[1]), "+v"(x_[2]), "+v"(x_[3])); __builtin_amdgcn_sched_barrier(0); \
        AHEAD(t) \
        const float y0 = (b3 ? x_[2] : x_[0]) + dpp<XOR8>(b3 ? x_[0] : x_[2]), y1 = (b3 ? x_[3] : x_[1]) + dpp<XOR8>(b3 ? x_[1] : x_[3]); \
        z[t] = (b2 ? y1 : y0) + dpp<XOR7>(b2 ? y0 : y1); }
      KSTEP(0) KSTEP(1) KSTEP(2) KSTEP(3)
      const float u0 = (b1 ? z[2] : z[0]) + dpp<XOR2>(b1 ? z[0] : z[2]), u1 = (b1 ? z[3] : z[1]) + dpp<XOR2>(b1 ? z[1] : z[3]);
      const float s = rbf(rbf((b0 ? u1 : u0) + dpp<XOR1>(b0 ? u0 : u1)) * SCALE) * LOG2E;
      sc[u * 64] = s;
      mx = fmaxf(mx, s);
      oc = on;
      if (wave == 0) { __hip_atomic_store(ctl + C_BLK, u + 1, RLX_WG); if ((u & 3) == 3) STAMP_AT(16 + (u >> 2)); }
    }
    *(LAS float*)(ctl + C_MX + wave) = wave_max(mx);
    lds_add(ctl + C_M);
    lds_wait_ge<0>(ctl + C_M, 4);
    const f32x4 mw = *(LAS f32x4*)(ctl + C_MX);
    const float M = fmaxf(fmaxf(mw[0], mw[1]), fmaxf(mw[2], mw[3]));
    float l = 0.f;
#pragma unroll
    for (int u = NSU; u < 16; ++u) { const float p = untag(tagw(__builtin_amdgcn_exp2f(sc[u * 64] - M), 0)); l += p; sc[u * 64] = p; }
    lgkm0();
#pragma unroll
    for (int u4 = 0; u4 < NDU; u4 += 4) {
      const f32x4 pq = *(LAS f32x4*)((LAS float*)(sm + L_SC) + wave * 1024 + min(NSU + u4 + r, 15) * 64 + 4 * c);
      st_tag4(rw, u4 + r < NDU ? W_P + ((row0 * NDU + u4 + r) * 4 + wave) * 256 + c * 16 : OOB, pq, tag);
    }
    if (wave == 0) STAMP_AT(24);
    float acc[8];
#pragma unroll
    for (int k = 0; k < 8; ++k) acc[k] = 0.f;
#define PV1(n, i, pr) { const float pb = dpp<0x150 + 4 * (i) + (n)>(pr); _Pragma("unroll") for (int k = 0; k < 4; ++k) { \
      acc[2 * k] = fmaf(pb, bflo(a[n][i][k]), acc[2 * k]); acc[2 * k + 1] = fmaf(pb, bfhi(a[n][i][k]), acc[2 * k + 1]); } }
#define VSTEP(n, pr) PV1(n, 0, pr) PV1(n, 1, pr) PV1(n, 2, pr) PV1(n, 3, pr) \
      asm volatile("" : "+v"(acc[0]), "+v"(acc[1]), "+v"(acc[2]), "+v"(acc[3]), "+v"(acc[4]), "+v"(acc[5]), "+v"(acc[6]), "+v"(acc[7])); \
      __builtin_amdgcn_sched_barrier(0); AHEAD(n)
    for (int u = 0; u < NSU - 1; ++u) {
      const __amdgpu_buffer_rsrc_t rc = rv, rn = rv;
      const int on = oc + 65536;
      const float p = __builtin_amdgcn_exp2f(sc[u * 64] - M);
      l += p;
      VSTEP(0, p) VSTEP(1, p) VSTEP(2, p) VSTEP(3, p)
      oc = on;
      if (u == NSU - 2) __hip_atomic_store(ctl + C_P + wave, 1, RLX_WG);
      if (wave == 0) { __hip_atomic_store(ctl + C_BLK, u + 17, RLX_WG); if ((u & 3) == 3) STAMP_AT(20 + (u >> 2)); }
    }
    auto finish = [&](int step, int row, int slot) {
#pragma unroll
      for (int k = 0; k < 8; ++k) acc[k] = xrow_sum(acc[k]);
      LAS char* dp = sm + L_DUMP + (step & 3) * 4 * DUMP_REC;
      const bool up = r & 1;
      *(LAS f32x4*)(dp + wave * DUMP_REC + 16 + c * 32 + (r & 1) * 16) = f32x4{up ? acc[4] : acc[0], up ? acc[5] : acc[1], up ? acc[6] : acc[2], up ? acc[7] : acc[3]};
      if (step == 0) *(LAS float*)(dp + wave * DUMP_REC) = wave_sum(l);
      if ((lds_add(ctl + C_CNT + (step & 3)) & 3) == 3) {
        LAS char* src = dp + 16 + (lane & 31) * 16;
        f32x4 o = (*(LAS f32x4*)src + *(LAS f32x4*)(src + DUMP_REC)) + (*(LAS f32x4*)(src + 2 * DUMP_REC) + *(LAS f32x4*)(src + 3 * DUMP_REC));
        if (step == 0) {
          const float L = (*(LAS float*)dp + *(LAS float*)(dp + DUMP_REC)) + (*(LAS float*)(dp + 2 * DUMP_REC) + *(LAS float*)(dp + 3 * DUMP_REC));
          const float L_lo = L - untag(tagw(L, 0));
          if (lane >= 32) o = f32x4{L, L_lo, L, L_lo};
        }
        st_tag4(rw, lane < 32 ? W_REC + (row * NSLOT + slot) * 512 + lane * 16 : (lane == 32 && step == 0) ? W_L + row * 128 : OOB, o, tag);
        lgkm0();
      }
#pragma unroll
      for (int k = 0; k < 8; ++k) acc[k] = 0.f;
    };
    int t, on, pn_off;
    unsigned pn;
    __amdgpu_buffer_rsrc_t rn = rv;
    auto next_item = [&](int j) {
      for (unsigned s = 0; (t = lds_ld(ctl + C_TICK + j)) < 0 && s < (SPIN_CAP << 2); ++s) __builtin_amdgcn_s_sleep(1);
      __hip_atomic_store(ctl + C_P + wave, j + 2, RLX_WG);
      cfence();
      if (t < 0) t = NITEMS;
      const int tc = min(t, NITEMS - 1), row = bat * 32 + 4 * (tc / NDU) + (grp & 3);
      on = (row << 20) + (NSU + tc % NDU) * 65536;
      rn = __builtin_amdgcn_make_buffer_rsrc((void*)Vc, 0, t < NITEMS ? 1 << 28 : 0, 0x00020000);
      pn_off = t < NITEMS ? W_P + ((row * NDU + tc % NDU) * 4 + wave) * 256 + lane * 4 : OOB;
      pn = __builtin_amdgcn_raw_buffer_load_b32(rw, pn_off, 0, 16);
      __builtin_amdgcn_sched_barrier(0);
    };
#define VNEXT(n, pr, j) if ((n) + DIST == 4) next_item(j); VSTEP(n, pr)
    {
      const __amdgpu_buffer_rsrc_t rc = rv;
      const float p = __builtin_amdgcn_exp2f(sc[(NSU - 1) * 64] - M);
      l += p;
      VNEXT(0, p, 0) VNEXT(1, p, 0) VNEXT(2, p, 0) VNEXT(3, p, 0)
      if (wave == 0) STAMP_AT(2);
      finish(0, row0, 0);
    }
    for (int j = 1; t < NITEMS; ++j) {
      const __amdgpu_buffer_rsrc_t rc = rv;
      const int tc = t, oc = on;
      if (__builtin_expect(!__all(((pn ^ tag) & 0xffu) == 0u), 0)) {
        for (unsigned s = 0; s < SPIN_CAP; ++s) {
          __builtin_amdgcn_s_sleep(8);
          pn = __builtin_amdgcn_raw_buffer_load_b32(rw, pn_off, 0, 16);
          cfence();
          if (__all(((pn ^ tag) & 0xffu) == 0u)) break;
        }
      }
      const float p = untag(pn);
      VNEXT(0, p, j) VNEXT(1, p, j) VNEXT(2, p, j) VNEXT(3, p, j)
      finish(j, bat * 32 + 4 * (tc / NDU) + (grp & 3), 1 + tc % NDU);
    }
    lds_add(ctl + C_SDONE);
    if (wave == 0) STAMP_AT(3);
    return;
  }

  const int hw = wid < 8 ? wid : wid - 8;
  u64 tag64 = 0;
  unsigned tag;
  if (hw < 4) {
    __builtin_amdgcn_s_setprio(3);
    int vzero;
    asm volatile("v_mov_b32 %0, 0" : "=v"(vzero));
    u64 epoch_v = __hip_atomic_load((GAS u64*)(ws + W_EPOCH + vzero), RLX_AGENT);
    __builtin_amdgcn_sched_barrier(0);
    const __amdgpu_buffer_rsrc_t rq = __builtin_amdgcn_make_buffer_rsrc((void*)wq, 0, D_ * HD * 2, 0x00020000);
    u32x4 w[8];
#pragma unroll
    for (int i = 0; i < 8; ++i) w[i] = __builtin_amdgcn_raw_buffer_load_b128(rq, r * 8192 + c * 16, (128 * bat + 32 * hw + 4 * i) * 8192 + head * 256, WQ_AUX);
    wg_barrier();
    asm volatile("" : "+v"(epoch_v));
#pragma unroll
    for (int i = 0; i < 8; ++i) *(LAS u32x4*)(sm + L_W + (c >> 1) * W_TS + rowpos(32 * hw + 4 * i + r) * 32 + (c & 1) * 16) = w[i];
    tag64 = (((u64)(unsigned)__builtin_amdgcn_readfirstlane((int)(epoch_v >> 32)) << 32) | (unsigned)__builtin_amdgcn_readfirstlane((int)epoch_v)) + 1;
    asm volatile("" :: "s"(tag64));
    tag = (unsigned)tag64 & 0xffu;
    if (hw == 0) STAMP_AT(12);
    wg_barrier();
    if (hw == 0) { ctl[C_TAG] = (int)tag; STAMP_AT(10); }
    f32x4 d0 = {0.f, 0.f, 0.f, 0.f}, d1 = d0;
#pragma unroll
    for (int s = 0; s < 4; ++s) {
      const bf16x8 xf = *(LAS bf16x8*)(sm + L_XN + (lane & 7) * XN_STR + (128 * bat + 32 * s + 8 * r) * 2);
      d0 = __builtin_amdgcn_mfma_f32_16x16x32_bf16(ld_bfrag(sm + L_W + (2 * hw) * W_TS, 32 * s, lane), xf, d0, 0, 0, 0);
      d1 = __builtin_amdgcn_mfma_f32_16x16x32_bf16(ld_bfrag(sm + L_W + (2 * hw + 1) * W_TS, 32 * s, lane), xf, d1, 0, 0, 0);
    }
    st_tag4(rw, W_QS + (((c & 7) * 32 + head) * 8 + bat) * 512 + (32 * hw + 2 * (c & 8) + 4 * r) * 4, (c & 8) ? d1 : d0, tag);
    lds_add(ctl + C_ST);
    if (hw == 0) STAMP_AT(11);
    lds_wait_ge<0>(ctl + C_K23, 4);
    if (hw == 0) STAMP_AT(22);
    u32x4 g;
    for (unsigned s = 0; s < SPIN_CAP; ++s) {
      g = ld_tag4(rw, W_QS + (bat * 32 + head) * 4096 + hw * 1024 + lane * 16);
      if (s == 0) lds_add(ctl + C_GI);
      cfence();
      if (__all(tag_ok(g, tag))) break;
    }
    if (hw == 0) STAMP_AT(23);
    *(LAS f32x4*)(sm + L_QP + hw * 1024 + lane * 16) = untag4(g);
    lds_add(ctl + C_Q);
    if (hw != 0) __builtin_amdgcn_s_setprio(H_PRIO);
  } else {
    const int gw = hw - 4;
    __builtin_amdgcn_s_setprio(H_PRIO);
    {
      const u32x4 xa = *(const u32x4*)(x + hw * D_ + lane * 8), xb = *(const u32x4*)(x + hw * D_ + 512 + lane * 8);
      wg_barrier();
      if (gw < 3) ctl[gw * 64 + lane] = gw ? -1 : 0;
      ln_row(xa, xb, sm + L_XN + hw * XN_STR, lane);
    }
    wg_barrier();
    if (xcd != (grp & 7)) {
      const int fb = grp * 7 + xcd - (xcd > (grp & 7));
      const int cb = fb >> 2, ks = fb & 3, row0 = 256 * ks + 64 * gw;
      const char* wp = ((lane & 8) ? wu : wg) + (size_t)(row0 + 8 * r) * (F_ * 2) + cb * 128 + (lane & 7) * 16;
      lds_wait_ge<1>(ctl + C_GI, 4);
      u32x4 w[16];
#pragma unroll
      for (int i = 0; i < 16; ++i) { w[i] = __builtin_nontemporal_load((const u32x4*)(wp + (32 * (i >> 3) + (i & 7)) * (F_ * 2))); __builtin_amdgcn_sched_barrier(0); }
      tag = (unsigned)lds_ld(ctl + C_TAG);
      f32x4 d[8];
#pragma unroll
      for (int j = 0; j < 8; ++j) d[j] = f32x4{0.f, 0.f, 0.f, 0.f};
#pragma unroll
      for (int s = 0; s < 2; ++s) {
        const bf16x8 af = *(LAS bf16x8*)(sm + L_XN + (lane & 7) * XN_STR + (row0 + 32 * s + 8 * r) * 2);
#pragma unroll
        for (int j = 0; j < 8; ++j) {
          u32x4 bq;
#pragma unroll
          for (int mm = 0; mm < 4; ++mm) bq[mm] = __builtin_amdgcn_perm(w[8 * s + 2 * mm + 1][j >> 1], w[8 * s + 2 * mm][j >> 1], (j & 1) ? 0x07060302 : 0x05040100);
          d[j] = __builtin_amdgcn_mfma_f32_16x16x32_bf16(af, __builtin_bit_cast(bf16x8, bq), d[j], 0, 0, 0);
        }
      }
      {
        const int b0_ = 4 * (r & 1) + 2 * (r >> 1);
        const bool hi = r >> 1;
        LAS char* dst = sm + L_GUP + gw * 4096 + b0_ * 512 + c * 32;
#pragma unroll
        for (int e = 0; e < 2; ++e) {
          *(LAS f32x4*)(dst + e * 512) = f32x4{hi ? d[0][2 + e] : d[0][e], hi ? d[1][2 + e] : d[1][e], hi ? d[2][2 + e] : d[2][e], hi ? d[3][2 + e] : d[3][e]};
          *(LAS f32x4*)(dst + e * 512 + 16) = f32x4{hi ? d[4][2 + e] : d[4][e], hi ? d[5][2 + e] : d[5][e], hi ? d[6][2 + e] : d[6][e], hi ? d[7][2 + e] : d[7][e]};
        }
      }
      lds_add(ctl + C_GU);
      lds_wait_ge<1>(ctl + C_GU, 4);
      {
        LAS char* src = sm + L_GUP + ((lane >> 2) & 7) * 512 + (lane >> 5) * 256 + gw * 64 + (lane & 3) * 16;
        const f32x4 sum = (*(LAS f32x4*)src + *(LAS f32x4*)(src + 4096)) + (*(LAS f32x4*)(src + 8192) + *(LAS f32x4*)(src + 12288));
        st_tag4(rw, W_GS + ((4 * cb + gw) * 4 + ks) * 1024 + lane * 16, sum, tag);
      }
      if (gw == 3) STAMP_AT(28);
      if (gw == 1) {
        lds_wait_ge<32>(ctl + C_BLK, WAIT_G2);
        nap((blockIdx.x * 5) & (STAGGER - 1));
        u32x4 g[4];
        for (unsigned s = 0; s < SPIN_CAP; ++s) {
#pragma unroll
          for (int i = 0; i < 4; ++i) g[i] = ld_tag4(rw, W_GS + fb * 4096 + i * 1024 + lane * 16);
          cfence();
          if (__all(tag_ok(g[0], tag) && tag_ok(g[1], tag) && tag_ok(g[2], tag) && tag_ok(g[3], tag))) break;
          __builtin_amdgcn_s_sleep(32);
        }
        const f32x4 sv = (untag4(g[0]) + untag4(g[1])) + (untag4(g[2]) + untag4(g[3]));
        f32x4 h;
#pragma unroll
        for (int k = 0; k < 4; ++k) {
          auto t = __builtin_amdgcn_permlane32_swap(__float_as_uint(sv[k]), __float_as_uint(sv[k]), false, false);
          h[k] = rbf(rbf(__uint_as_float(t[0])) * rbf(__uint_as_float(t[1])));
        }
        st_tag4(rw, lane < 32 ? W_HG + (fb / 7) * 4096 + (lane >> 2) * 512 + (fb % 7) * 64 + (lane & 3) * 16 : OOB, h, tag);
        STAMP_AT(4);
      }
    } else {
      lds_wait_ge<1>(ctl + C_GI, 4);
      tag = (unsigned)lds_ld(ctl + C_TAG);
    }
  }
  GAS u64* const cnt = (GAS u64*)(ws + W_CNT + grp * 256);
  if (hw == 0) {
    lds_wait_ge<8>(ctl + C_QG, 1);
    int last = 0;
    for (int got = 0;; ++got) {
      for (unsigned s = 0; s < (SPIN_CAP << 2); ++s) {
        const int p0 = lds_ld(ctl + C_P), p1 = lds_ld(ctl + C_P + 1), p2 = lds_ld(ctl + C_P + 2), p3 = lds_ld(ctl + C_P + 3);
        if (max(max(p0, p1), max(p2, p3)) + (last < LOOK2 ? 1 : 0) > got && max(max(p0, p1), max(p2, p3)) > 0 && min(min(p0, p1), min(p2, p3)) >= got - SKEW - (last < LOOK2 && SKEW < 1 ? 1 : 0)) break;
        __builtin_amdgcn_s_sleep(PROG_SLEEP);
      }
      int t = 0;
      if (lane == 0) t = (int)(__hip_atomic_fetch_add(cnt, 1ull, RLX_AGENT) & 0xffff);
      t = __builtin_amdgcn_readfirstlane(t);
      if (lane == 0) __hip_atomic_store(ctl + C_TICK + got, t, RLX_WG);
      last = t;
      if (t >= NITEMS || got >= NITEMS) break;
    }
    lds_add(ctl + C_WC);
    STAMP_AT(5);
  } else {
    if (hw == 3) {
      const u64 fresh = tag64 << 16;
      lds_wait_ge<32>(ctl + C_BLK, WAIT_Q);
      nap((blockIdx.x * 11) & (STAGGER - 1));
      if (lane == 0) {
        u64 v = __hip_atomic_load(cnt, RLX_AGENT);
        if ((v >> 16) != (fresh >> 16)) __hip_atomic_compare_exchange_strong(cnt, &v, fresh, __ATOMIC_RELAXED, RLX_AGENT);
        asm volatile("" :: "v"(v));
      }
      asm volatile("s_waitcnt vmcnt(0)" ::: "memory");
      lds_add(ctl + C_QG);
    }
    if (hw == 2) {
      lds_wait_ge<32>(ctl + C_BLK, WAIT_H);
      nap((blockIdx.x * 3) & (STAGGER - 1));
      for (unsigned s = 0; s < SPIN_CAP; ++s) {
        u32x4 g[4];
        const bool live = (lane & 31) < 28;
#pragma unroll
        for (int i = 0; i < 4; ++i) g[i] = ld_tag4(rw, live ? W_HG + head * 4096 + i * 1024 + lane * 16 : OOB);
        cfence();
#pragma unroll
        for (int i = 0; i < 4; ++i) *(LAS u32x2*)(sm + L_HA + (2 * i + (lane >> 5)) * A_STR + (lane & 31) * 8) = hi16x4(g[i]);
        if (__all(!live || (tag_ok(g[0], tag) && tag_ok(g[1], tag) && tag_ok(g[2], tag) && tag_ok(g[3], tag)))) break;
        __builtin_amdgcn_s_sleep(64);
      }
    }
    if (hw >= 4) {
      lds_wait_ge<32>(ctl + C_BLK, W_BLK);
      nap((blockIdx.x * 7) & (STAGGER - 1));
      const __amdgpu_buffer_rsrc_t ro = __builtin_amdgcn_make_buffer_rsrc((void*)wo, 0, HD * D_ * 2, 0x00020000);
      const __amdgpu_buffer_rsrc_t rd = __builtin_amdgcn_make_buffer_rsrc((void*)wd, 0, F_ * D_ * 2, 0x00020000);
      u32x4 wv[15];
#pragma unroll
      for (int i = 0; i < 15; ++i)
        wv[i] = __builtin_amdgcn_raw_buffer_load_b128(i < 8 ? ro : rd, r * 2048 + c * 16, ((i < 8 ? 128 * head : 112 * head - 128) + 4 * (hw - 4) + 16 * i) * 2048 + bat * 256, W_AUX);
#pragma unroll
      for (int i = 0; i < 15; ++i) *(LAS u32x4*)(sm + L_W + (c >> 1) * W_TS + rowpos(4 * (hw - 4) + 16 * i + r) * 32 + (c & 1) * 16) = wv[i];
    }
    lds_add(ctl + C_WC);
  }
  f32x4 dacc = {0.f, 0.f, 0.f, 0.f};
  bf16x8 bo[4];
  {
    lds_wait_ge<4>(ctl + C_WC, 8);
    LAS char* img = sm + L_W + hw * W_TS;
#pragma unroll
    for (int s = 0; s < 4; ++s) bo[s] = ld_bfrag(img, 32 * s, lane);
#pragma unroll
    for (int s = 0; s < 3; ++s)
      dacc = __builtin_amdgcn_mfma_f32_16x16x32_bf16(ld_bfrag(img, 128 + 32 * s, lane), *(LAS bf16x8*)(sm + L_HA + (lane & 7) * A_STR + (32 * s + 8 * r) * 2), dacc, 0, 0, 0);
    dacc = __builtin_amdgcn_mfma_f32_16x16x16bf16_1k(ld_tr(img + rowpos(224 + 4 * r + ((lane >> 2) & 3)) * 32 + (lane & 3) * 8),
                                                     *(LAS s16x4*)(sm + L_HA + (lane & 7) * A_STR + (96 + 4 * r) * 2), dacc, 0, 0, 0);
  }
  const int rchunk = blockIdx.x >> 3, rb = blockIdx.x & 7;
  u32x2 xres = {0u, 0u};
  if (hw == 0) xres = *(const u32x2*)(x + rb * D_ + rchunk * 32 + (lane & 7) * 4);
  if (hw == 0) STAMP_AT(6);
  lds_wait_ge<1>(ctl + C_SDONE, 4);
  if (hw == 0) STAMP_AT(7);
  {
    const int row = hw * 32 + head;
    constexpr int NL = (NSLOT + 1) / 2;
    u32x4 g[NL], e;
    const bool odd = lane >> 5;
    for (unsigned s = 0; s < SPIN_CAP; ++s) {
      e = ld_tag4(rw, W_L + row * 128);
#pragma unroll
      for (int i = 0; i < NL; ++i) g[i] = ld_tag4(rw, (2 * i + 1 < NSLOT || !odd) ? W_REC + row * NSLOT * 512 + i * 1024 + lane * 16 : OOB);
      cfence();
      bool ok = tag_ok(e, tag);
#pragma unroll
      for (int i = 0; i < NL; ++i) ok &= tag_ok(g[i], tag) || (2 * i + 1 >= NSLOT && odd);
      if (__all(ok)) break;
      __builtin_amdgcn_s_sleep(2);
    }
    f32x4 o = untag4(g[0]);
#pragma unroll
    for (int i = 1; i < NL; ++i) o += untag4(g[i]);
    const float inv = 1.f / (untag(e[0]) + untag(e[1]));
#pragma unroll
    for (int k = 0; k < 4; ++k) o[k] = x32_sum(o[k]) * inv;
    *(LAS u32x2*)(sm + L_OA + hw * A_STR + (lane & 31) * 8) = u32x2{pack2(o[0], o[1]), pack2(o[2], o[3])};
  }
  lds_add(ctl + C_OA);
  lds_wait_ge<0>(ctl + C_OA, 8);
  if (hw == 0) STAMP_AT(8);
#pragma unroll
  for (int s = 0; s < 4; ++s)
    dacc = __builtin_amdgcn_mfma_f32_16x16x32_bf16(bo[s], *(LAS bf16x8*)(sm + L_OA + (lane & 7) * A_STR + (32 * s + 8 * r) * 2), dacc, 0, 0, 0);
  st_tag4(rw, (c & 8) ? OOB : W_PART + (((4 * bat + (hw >> 1)) * 8 + (c & 7)) * 32 + head) * 128 + (16 * (hw & 1) + 4 * r) * 4, dacc, tag);
  if (hw != 0) return;
  {
    u32x4 g[4];
    for (unsigned s = 0; s < SPIN_CAP; ++s) {
#pragma unroll
      for (int k = 0; k < 4; ++k) g[k] = ld_tag4(rw, W_PART + (rchunk * 8 + rb) * 4096 + k * 1024 + lane * 16);
      cfence();
      if (__all(tag_ok(g[0], tag) && tag_ok(g[1], tag) && tag_ok(g[2], tag) && tag_ok(g[3], tag))) break;
      __builtin_amdgcn_s_sleep(1);
    }
    f32x4 sum = (untag4(g[0]) + untag4(g[1])) + (untag4(g[2]) + untag4(g[3]));
#pragma unroll
    for (int k = 0; k < 4; ++k) { float t = sum[k]; t += dpp<XOR8>(t); sum[k] = xrow_sum(t); }
    if (lane < 8)
      *(u32x2*)(out + rb * D_ + rchunk * 32 + lane * 4) = u32x2{pack2(bflo(xres[0]) + sum[0], bfhi(xres[0]) + sum[1]), pack2(bflo(xres[1]) + sum[2], bfhi(xres[1]) + sum[3])};
    if (blockIdx.x == 0 && lane == 0) __hip_atomic_store((GAS u64*)(ws + W_EPOCH), tag64, RLX_AGENT);
    STAMP_AT(9);
    STAMP_DUMP();
  }
}

extern "C" void kernel_launch(void* const* d_in, const int* in_sizes, int n_in, void* d_out, int out_size, void* d_ws, size_t ws_size,
                              hipStream_t stream) {
  fused_decode<<<256, NTHR, 0, stream>>>((const unsigned short*)d_in[0], (const char*)d_in[1], (const char*)d_in[2], (const char*)d_in[3],
                                         (const char*)d_in[4], (const char*)d_in[6], (const char*)d_in[7], (const char*)d_in[8],
                                         (unsigned short*)d_out, (char*)d_ws + ((ws_size - (8u  __H0__ )) & ~(size_t)4095));
}
```

```cpp
#include <hip/hip_runtime.h>
#include <hip/hip_bf16.h>
#include <stdint.h>
#include <type_traits>

typedef unsigned u32x4 __attribute__((ext_vector_type(4)));
typedef unsigned u32x2 __attribute__((ext_vector_type(2)));
typedef float f32x4 __attribute__((ext_vector_type(4)));
typedef float f32x2 __attribute__((ext_vector_type(2)));
typedef short s16x4 __attribute__((ext_vector_type(4)));
typedef short s16x8 __attribute__((ext_vector_type(8)));
typedef __bf16 bf16x2 __attribute__((ext_vector_type(2)));
typedef __bf16 bf16x8 __attribute__((ext_vector_type(8)));
typedef unsigned long long u64;
#define GAS __attribute__((address_space(1)))
#define LAS __attribute__((address_space(3)))
#define RLX_AGENT __ATOMIC_RELAXED, __HIP_MEMORY_SCOPE_AGENT
#define RLX_WG __ATOMIC_RELAXED, __HIP_MEMORY_SCOPE_WORKGROUP

constexpr int D_ = 1024, SK = 4096, DH = 128, F_ = 3584, HD = 4096;
constexpr int NTHR = 768;
#define NB2_ 2
#define NS1_ 2
constexpr int NB2 = NB2_, NS1 = NS1_, NDU = 2 * NB2 + NS1, NSU = 16 - NDU, NP = NDU / 2;
static_assert(NS1 % 2 == 0 && NP <= 4, "unit pairs");
constexpr int NITEMS = 8 * (NB2 + NS1);
constexpr int NSLOT = 1 + NB2 + NS1;
constexpr unsigned SPIN_CAP = 1u << 17;
#define WQ_AUX 2
#define K_AUX 2
#define V_AUX 2
#define W_AUX 2
#define GU_AUX 2
#define S_PRIO 0
#define H_PRIO 2
#define END_SLEEP 8
#define PROG_SLEEP 2
#define WAIT_G2 1
#define WAIT_Q 10
#define WAIT_H 4
#define STAGGER 1
#define REC_SLEEP 1
#define POLL_SLEEP 1
#define P_GRID 9
#define LOOK2 0
#define WS_OFF 0
#define SKEW -1
#define DIST 3
#define ORDER 0
#define Q_NAP 8
#define G_FIRST 0
#define K_FIRST 3

constexpr int L_XN = 0, XN_STR = 2064;
constexpr int L_QP = L_XN + 8 * XN_STR;
constexpr int L_GUP = L_QP + 4096;
constexpr int L_SC = L_GUP + 16384;
constexpr int L_DUMP = L_SC + 16384;
constexpr int L_LS = L_DUMP + 4 * 8192;
constexpr int L_W = L_LS + 1024, W_TS = 8224;
constexpr int L_OA = L_W + 8 * W_TS, A_STR = 272;
constexpr int L_HA = L_OA + 8 * A_STR;
constexpr int L_CTL = L_HA + 8 * A_STR;
constexpr int C_TICK = 64, C_LD = 28, C_PL = 32, C_PUB = 33, C_MX = 36, C_M = 40, C_P = 44, C_QG = 48, C_CNT = 49, C_GU = 53, C_WC = 54, C_OA = 55, C_END = 56, C_SDONE = 57, C_TAG = 58, C_ST = 59, C_K23 = 60, C_GI = 61, C_BLK = 62, C_Q = 63, C_WORDS = 64 + 128;
constexpr int L_BYTES = L_CTL + C_WORDS * 4;
constexpr int REC_ROW = (NSLOT / 2 + 1) * 1024;
constexpr unsigned W_EPOCH = 0, W_CNT = 4096, W_P = 16384, W_HG = W_P + 256 * NP * 1024, W_REC = W_HG + 32 * 4096,
                   W_PART = W_REC + 256 * REC_ROW, W_QS = W_PART + 256 * 4096, W_GS = W_QS + 256 * 4096, W_END = W_GS + 224 * 4096, W_STAMP = 8u << 20;
static_assert(W_END <= W_STAMP, "workspace map");
constexpr int OOB = 0x7ffffff0;

__device__ __forceinline__ float bflo(unsigned x) { return __builtin_bit_cast(float, x << 16); }
__device__ __forceinline__ float bfhi(unsigned x) { return __builtin_bit_cast(float, x & 0xffff0000u); }
__device__ __forceinline__ float rbf(float x) { return (float)(__bf16)x; }
__device__ __forceinline__ unsigned pack2(float lo, float hi) { bf16x2 v = {(__bf16)lo, (__bf16)hi}; return __builtin_bit_cast(unsigned, v); }
__device__ __forceinline__ float dot2(unsigned k, unsigned q, float acc) {
  return __builtin_amdgcn_fdot2_f32_bf16(__builtin_bit_cast(bf16x2, k), __builtin_bit_cast(bf16x2, q), acc, false);
}
template <int CTRL> __device__ __forceinline__ float dpp(float x) {
  return __builtin_bit_cast(float, __builtin_amdgcn_mov_dpp(__builtin_bit_cast(int, x), CTRL, 0xf, 0xf, true));
}
constexpr int XOR1 = 0xB1, XOR2 = 0x4E, XOR7 = 0x141, XOR8 = 0x128;
__device__ __forceinline__ float xrow_max(float x) {
  auto s = __builtin_amdgcn_permlane16_swap(__float_as_uint(x), __float_as_uint(x), false, false);
  x = fmaxf(__uint_as_float(s[0]), __uint_as_float(s[1]));
  auto t = __builtin_amdgcn_permlane32_swap(__float_as_uint(x), __float_as_uint(x), false, false);
  return fmaxf(__uint_as_float(t[0]), __uint_as_float(t[1]));
}
__device__ __forceinline__ float x32_sum(float x) {
  auto t = __builtin_amdgcn_permlane32_swap(__float_as_uint(x), __float_as_uint(x), false, false);
  return __uint_as_float(t[0]) + __uint_as_float(t[1]);
}
__device__ __forceinline__ float xrow_sum(float x) {
  auto s = __builtin_amdgcn_permlane16_swap(__float_as_uint(x), __float_as_uint(x), false, false);
  return x32_sum(__uint_as_float(s[0]) + __uint_as_float(s[1]));
}
__device__ __forceinline__ float wave_sum(float x) {
  x += dpp<XOR1>(x); x += dpp<XOR2>(x); x += dpp<XOR7>(x); x += dpp<XOR8>(x); return xrow_sum(x);
}
__device__ __forceinline__ float wave_max(float x) {
  x = fmaxf(x, dpp<XOR1>(x)); x = fmaxf(x, dpp<XOR2>(x)); x = fmaxf(x, dpp<XOR7>(x)); x = fmaxf(x, dpp<XOR8>(x)); return xrow_max(x);
}
__device__ __forceinline__ void lgkm0() { asm volatile("s_waitcnt lgkmcnt(0)" ::: "memory"); }
__device__ __forceinline__ void cfence() { asm volatile("" ::: "memory"); }
__device__ __forceinline__ void wg_barrier() { asm volatile("s_waitcnt lgkmcnt(0)\n\ts_barrier" ::: "memory"); }
__device__ __forceinline__ int lds_ld(LAS int* p) { return __builtin_amdgcn_readfirstlane(__hip_atomic_load(p, RLX_WG)); }
__device__ __forceinline__ int lds_add(LAS int* p) {
  int old; u64 keep;
  asm volatile("s_waitcnt lgkmcnt(0)\n\ts_mov_b64 %1, exec\n\ts_mov_b64 exec, 1\n\tds_add_rtn_u32 %0, %2, %3\n\ts_mov_b64 exec, %1\n\ts_waitcnt lgkmcnt(0)"
               : "=&v"(old), "=&s"(keep) : "v"(p), "v"(1) : "memory");
  return __builtin_amdgcn_readfirstlane(old);
}
template <int SLEEP> __device__ __forceinline__ void lds_wait_ge(LAS int* p, int want) {
  for (unsigned s = 0; lds_ld(p) < want && s < (SPIN_CAP << 2); ++s) __builtin_amdgcn_s_sleep(SLEEP);
  cfence();
}
template <unsigned TM = 0xffu> __device__ __forceinline__ unsigned tagw(float v, unsigned tag) { return ((__float_as_uint(v) + (TM + 1) / 2) & ~TM) | (tag & TM); }
template <unsigned TM = 0xffu> __device__ __forceinline__ float untag(unsigned w) { return __uint_as_float(w & ~TM); }
template <unsigned TM = 0xffu> __device__ __forceinline__ void st_tag4(__amdgpu_buffer_rsrc_t rw, int off, f32x4 v, unsigned tag) {
  __builtin_amdgcn_raw_buffer_store_b128(u32x4{tagw<TM>(v[0], tag), tagw<TM>(v[1], tag), tagw<TM>(v[2], tag), tagw<TM>(v[3], tag)}, rw, off, 0, 16);
}
__device__ __forceinline__ u32x4 ld_tag4(__amdgpu_buffer_rsrc_t rw, int off) { return __builtin_amdgcn_raw_buffer_load_b128(rw, off, 0, 16); }
template <unsigned TM = 0xffu> __device__ __forceinline__ bool tag_ok(u32x4 w, unsigned tag) { return (((w[0] ^ tag) | (w[1] ^ tag) | (w[2] ^ tag) | (w[3] ^ tag)) & TM) == 0u; }
template <unsigned TM = 0xffu> __device__ __forceinline__ f32x4 untag4(u32x4 w) { return f32x4{untag<TM>(w[0]), untag<TM>(w[1]), untag<TM>(w[2]), untag<TM>(w[3])}; }
__device__ __forceinline__ u32x2 hi16x4(u32x4 w) { return u32x2{__builtin_amdgcn_perm(w[1], w[0], 0x07060302), __builtin_amdgcn_perm(w[3], w[2], 0x07060302)}; }
template <int N> __device__ __forceinline__ void gather(__amdgpu_buffer_rsrc_t rw, int (&off)[N], u32x4 (&g)[N], unsigned tag, int lane) {
#pragma unroll
  for (int i = 0; i < N; ++i) g[i] = u32x4{0u, 0u, 0u, 0u};
  for (unsigned s = 0; s < SPIN_CAP; ++s) {
    u32x4 v[N];
#pragma unroll
    for (int i = 0; i < N; ++i) v[i] = ld_tag4(rw, off[i]);
    cfence();
    bool done = true;
#pragma unroll
    for (int i = 0; i < N; ++i) {
      const u64 good = __ballot(tag_ok(v[i], tag) || off[i] == OOB);
      if (((unsigned)(good >> (lane & 56)) & 0xffu) == 0xffu && off[i] != OOB) { g[i] = v[i]; off[i] = OOB; }
      done &= off[i] == OOB;
    }
    if (__all(done)) break;
    __builtin_amdgcn_s_sleep(POLL_SLEEP);
  }
}
__device__ __forceinline__ void nap(int n) { for (int i = 0; i < n; ++i) __builtin_amdgcn_s_sleep(16); }
struct Item { int rowi, pair, half, slot, two; };
__device__ __forceinline__ Item item_of(int t) {
  Item it;
  it.two = t < 8 * NB2;
  const int k = it.two ? t : t - 8 * NB2, n = it.two ? NB2 : NS1;
  it.rowi = NB2 == NS1 ? k / NS1 : it.two ? k / (NB2 ? NB2 : 1) : k / NS1;
  const int e = k - it.rowi * n;
  it.pair = it.two ? e : NB2 + (e >> 1);
  it.half = it.two ? 0 : e & 1;
  it.slot = it.two ? e : NB2 + e;
  return it;
}
__device__ __forceinline__ unsigned enc13(float p) { return (unsigned)max((int)(pack2(p, 0.f) & 0xffffu) - 8192, 0); }
__device__ __forceinline__ float dec13(unsigned v) { v &= 0x1fffu; return __uint_as_float(v ? (v + 8192u) << 16 : 0u); }
__device__ __forceinline__ void lds_inc(LAS int* p) {
  u64 keep;
  asm volatile("s_waitcnt lgkmcnt(0)\n\ts_mov_b64 %0, exec\n\ts_mov_b64 exec, 1\n\tds_add_u32 %1, %2\n\ts_mov_b64 exec, %0" : "=&s"(keep) : "v"(p), "v"(1) : "memory");
}
__device__ __forceinline__ int rowpos(int row) { return row ^ ((row >> 1) & 4); }
__device__ __forceinline__ s16x4 ld_tr(LAS char* p) { return __builtin_amdgcn_ds_read_tr16_b64_v4i16((LAS s16x4*)p); }
__device__ __forceinline__ bf16x8 ld_bfrag(LAS char* img, int row0, int lane) {
  const int g = lane >> 4, q = (lane >> 2) & 3, p = lane & 3;
  const s16x4 lo = ld_tr(img + rowpos(row0 + 8 * g + q) * 32 + p * 8), hi = ld_tr(img + rowpos(row0 + 8 * g + 4 + q) * 32 + p * 8);
  return __builtin_bit_cast(bf16x8, __builtin_shufflevector(lo, hi, 0, 1, 2, 3, 4, 5, 6, 7));
}
#define STAMP_AT(i) do { } while (0)
#define STAMP_VAL(i, v) do { } while (0)
#define STAMP_DUMP() do { } while (0)
#define USTAMP(i) do { } while (0)
#define L_EXTRA 0

__device__ __forceinline__ void ln_row(const u32x4 xa, const u32x4 xb, LAS char* dst, int lane) {
  float v[16];
#pragma unroll
  for (int j = 0; j < 4; ++j) { v[2 * j] = bflo(xa[j]); v[2 * j + 1] = bfhi(xa[j]); v[8 + 2 * j] = bflo(xb[j]); v[8 + 2 * j + 1] = bfhi(xb[j]); }
  float s = 0.f;
#pragma unroll
  for (int j = 0; j < 16; ++j) s += v[j];
  const float mean = wave_sum(s) * (1.f / D_);
  float q = 0.f;
#pragma unroll
  for (int j = 0; j < 16; ++j) { const float d = v[j] - mean; q = fmaf(d, d, q); }
  const float var = wave_sum(q) * (1.f / D_);
  const float mb = rbf(mean), rs = 1.f / rbf(sqrtf(rbf(rbf(var) + 1e-5f)));
  u32x4 o0, o1;
#pragma unroll
  for (int j = 0; j < 4; ++j) {
    o0[j] = pack2(rbf(v[2 * j] - mb) * rs, rbf(v[2 * j + 1] - mb) * rs);
    o1[j] = pack2(rbf(v[8 + 2 * j] - mb) * rs, rbf(v[8 + 2 * j + 1] - mb) * rs);
  }
  *(LAS u32x4*)(dst + lane * 16) = o0;
  *(LAS u32x4*)(dst + 1024 + lane * 16) = o1;
}

__global__ void __launch_bounds__(NTHR, 3)
fused_decode(const unsigned short* __restrict__ x, const char* Kc, const char* Vc, const char* __restrict__ wq,
             const char* __restrict__ wo, const char* __restrict__ wg, const char* __restrict__ wu, const char* __restrict__ wd,
             unsigned short* __restrict__ out, char* ws) {
  __shared__ __attribute__((aligned(16))) char smem_[L_BYTES + L_EXTRA];
  LAS char* const sm = (LAS char*)smem_;
  LAS int* const ctl = (LAS int*)(sm + L_CTL);
  const int tid = threadIdx.x, lane = tid & 63, wid0 = __builtin_amdgcn_readfirstlane(tid >> 6), r = lane >> 4, c = lane & 15;
  const int wid = ORDER == 0 ? wid0 : ORDER == 1 ? (wid0 < 4 ? wid0 + 8 : wid0 - 4) : (wid0 < 4 ? wid0 + 4 : wid0 < 8 ? wid0 + 4 : wid0 - 8);
  const int xcd = blockIdx.x & 7, grp = blockIdx.x >> 3;
  const int head = 4 * xcd + (grp & 3), bat = grp >> 2;

  const __amdgpu_buffer_rsrc_t rw = __builtin_amdgcn_make_buffer_rsrc((void*)ws, 0, W_END, 0x00020000);

  if (wid < 4) {
    const int wave = wid;
    const __amdgpu_buffer_rsrc_t rk = __builtin_amdgcn_make_buffer_rsrc((void*)Kc, 0, 1 << 28, 0x00020000);
    const __amdgpu_buffer_rsrc_t rv = __builtin_amdgcn_make_buffer_rsrc((void*)Vc, 0, 1 << 28, 0x00020000);
    const int voff = wave * 4096 + lane * 16;
    const int row0 = bat * 32 + head;
    u32x4 a[4][4];
#define ISSUE(n, rs, so, aux) _Pragma("unroll") for (int g_ = 0; g_ < 4; ++g_) { \
      a[n][g_] = __builtin_amdgcn_raw_buffer_load_b128(rs, voff + g_ * 1024, so, aux); __builtin_amdgcn_sched_barrier(0); }
#define AHEAD(t, aux) if ((t) + DIST < 4) { ISSUE((t) + DIST, rc, oc + ((t) + DIST) * 16384, aux) } else { ISSUE(((t) + DIST) & 3, rn, on + (((t) + DIST) & 3) * 16384, aux) }
    if (wave == 0) STAMP_AT(0);
    int oc = row0 << 20;
    {
      const u32x4 xa = *(const u32x4*)(x + wave * D_ + lane * 8), xb = *(const u32x4*)(x + wave * D_ + 512 + lane * 8);
      wg_barrier();
      ISSUE(0, rk, oc, K_AUX) ISSUE(1, rk, oc + 16384, K_AUX)
      if (K_FIRST > 2) { ISSUE(2, rk, oc + 32768, K_AUX) }
      ln_row(xa, xb, sm + L_XN + wave * XN_STR, lane);
    }
    if (wave == 0) STAMP_AT(13);
    wg_barrier();
    if (DIST > K_FIRST) {
      lds_wait_ge<0>(ctl + C_ST, 4);
      if (K_FIRST < 3 && DIST > 2) { ISSUE(2, rk, oc + 32768, K_AUX) }
      if (DIST > 3) { ISSUE(3, rk, oc + 49152, K_AUX) }
    }
    __builtin_amdgcn_s_setprio(S_PRIO);
    lds_wait_ge<0>(ctl + C_Q, 4);
    u32x4 qv;
    {
      float qs[8];
#pragma unroll
      for (int w = 0; w < 8; ++w) {
        const f32x4 p0 = *(LAS f32x4*)(sm + L_QP + w * 512 + c * 32), p1 = *(LAS f32x4*)(sm + L_QP + w * 512 + c * 32 + 16);
#pragma unroll
        for (int k = 0; k < 4; ++k) { qs[k] = w ? qs[k] + p0[k] : p0[k]; qs[4 + k] = w ? qs[4 + k] + p1[k] : p1[k]; }
      }
#pragma unroll
      for (int k = 0; k < 4; ++k) qv[k] = pack2(qs[2 * k], qs[2 * k + 1]);
    }
    const unsigned tag = (unsigned)lds_ld(ctl + C_TAG);
    if (wave == 0) STAMP_AT(1);
    const bool b3 = c & 8, b2 = c & 4, b1 = c & 2, b0 = c & 1;
    constexpr float SCALE = 0.08838834764831845f, LOG2E = 1.4426950408889634f;
    LAS float* const sc = (LAS float*)(sm + L_SC) + wave * 1024 + lane;
    float mx = -INFINITY;
    for (int u = 0; u < 16; ++u) {
      const __amdgpu_buffer_rsrc_t rc = rk, rn = __builtin_amdgcn_make_buffer_rsrc((void*)(u < 15 ? Kc : Vc), 0, 1 << 28, 0x00020000);
      const int on = u < 15 ? oc + 65536 : row0 << 20;
      float z[4];
#define KSTEP(t) { float x_[4]; _Pragma("unroll") for (int i = 0; i < 4; ++i) { float d = 0.f; \
        _Pragma("unroll") for (int k = 0; k < 4; ++k) d = dot2(a[t][i][k], qv[k], d); x_[i] = d; } \
        asm volatile("" : "+v"(x_[0]), "+v"(x_[1]), "+v"(x_[2]), "+v"(x_[3])); __builtin_amdgcn_sched_barrier(0); \
        AHEAD(t, K_AUX) \
        const float y0 = (b3 ? x_[2] : x_[0]) + dpp<XOR8>(b3 ? x_[0] : x_[2]), y1 = (b3 ? x_[3] : x_[1]) + dpp<XOR8>(b3 ? x_[1] : x_[3]); \
        z[t] = (b2 ? y1 : y0) + dpp<XOR7>(b2 ? y0 : y1); }
      KSTEP(0) KSTEP(1) KSTEP(2) KSTEP(3)
      const float u0 = (b1 ? z[2] : z[0]) + dpp<XOR2>(b1 ? z[0] : z[2]), u1 = (b1 ? z[3] : z[1]) + dpp<XOR2>(b1 ? z[1] : z[3]);
      const float s = rbf(rbf((b0 ? u1 : u0) + dpp<XOR1>(b0 ? u0 : u1)) * SCALE) * LOG2E;
      sc[u * 64] = s;
      mx = fmaxf(mx, s);
      oc = on;
      USTAMP(u);
      if (wave == 0) { __hip_atomic_store(ctl + C_BLK, u + 1, RLX_WG); if ((u & 3) == 3) STAMP_AT(16 + (u >> 2)); }
    }
    const float M = wave_max(mx);
    *(LAS float*)(ctl + C_MX + wave) = M;
    lds_add(ctl + C_M);
    float l = 0.f;
    if (wave == 0) STAMP_AT(24);
    float acc[8];
#pragma unroll
    for (int k = 0; k < 8; ++k) acc[k] = 0.f;
#define PV1(n, i, pr) { const float pb = dpp<0x150 + 4 * (i) + (n)>(pr); _Pragma("unroll") for (int k = 0; k < 4; ++k) { \
      acc[2 * k] = fmaf(pb, bflo(a[n][i][k]), acc[2 * k]); acc[2 * k + 1] = fmaf(pb, bfhi(a[n][i][k]), acc[2 * k + 1]); } }
#define VSTEP(n, pr) PV1(n, 0, pr) PV1(n, 1, pr) PV1(n, 2, pr) PV1(n, 3, pr) \
      asm volatile("" : "+v"(acc[0]), "+v"(acc[1]), "+v"(acc[2]), "+v"(acc[3]), "+v"(acc[4]), "+v"(acc[5]), "+v"(acc[6]), "+v"(acc[7])); \
      __builtin_amdgcn_sched_barrier(0); AHEAD(n, V_AUX)
    for (int u = 0; u < NSU - 1; ++u) {
      const __amdgpu_buffer_rsrc_t rc = rv, rn = rv;
      const int on = oc + 65536;
      const float p = __builtin_amdgcn_exp2f(sc[u * 64] - M);
      l += p;
      VSTEP(0, p) VSTEP(1, p) VSTEP(2, p) VSTEP(3, p)
      oc = on;
      if (u == NSU - 2) __hip_atomic_store(ctl + C_P + wave, 1, RLX_WG);
      USTAMP(u + 16);
      if (wave == 0) { __hip_atomic_store(ctl + C_BLK, u + 17, RLX_WG); if ((u & 3) == 3) STAMP_AT(20 + (u >> 2)); }
    }
    auto dump = [&](int step) {
      LAS char* dp = sm + L_DUMP + (step & 3) * 8192 + wave * 2048 + lane * 16;
      *(LAS f32x4*)dp = f32x4{acc[0], acc[1], acc[2], acc[3]};
      *(LAS f32x4*)(dp + 1024) = f32x4{acc[4], acc[5], acc[6], acc[7]};
      lds_inc(ctl + C_CNT + (step & 3));
#pragma unroll
      for (int k = 0; k < 8; ++k) acc[k] = 0.f;
    };
    int t, on, pn_off, nhalf = 0, ntwo = 0;
    unsigned pn = 0;
    __amdgpu_buffer_rsrc_t rn = rv;
    auto next_unit = [&](int j, int oc_) {
      const bool more = ntwo;
      if (more) {
        on = oc_ + 65536; nhalf = 1; ntwo = 0;
      } else {
        for (unsigned s = 0; (t = lds_ld(ctl + C_TICK + j)) < 0 && s < (SPIN_CAP << 2); ++s) __builtin_amdgcn_s_sleep(1);
        __hip_atomic_store(ctl + C_P + wave, j + 2, RLX_WG);
        cfence();
        if (t < 0) t = NITEMS;
        const Item it = item_of(min(t, NITEMS - 1));
        const int row = bat * 32 + 4 * it.rowi + (grp & 3);
        on = (row << 20) + (NSU + 2 * it.pair + it.half) * 65536;
        rn = __builtin_amdgcn_make_buffer_rsrc((void*)Vc, 0, t < NITEMS ? 1 << 28 : 0, 0x00020000);
        pn_off = W_P + ((row * NP + it.pair) * 4 + wave) * 256;
        nhalf = it.half; ntwo = it.two;
      }
      const unsigned pl = __builtin_amdgcn_raw_buffer_load_b32(rw, (more || t >= NITEMS) ? OOB : pn_off + lane * 4, 0, 16);
      __builtin_amdgcn_sched_barrier(0);
      return pl;
    };
#define VNEXT(n, pr, j) if ((n) + DIST == 4) pl = next_unit(j, oc); VSTEP(n, pr)
    unsigned pl;
    {
      const __amdgpu_buffer_rsrc_t rc = rv;
      const float p = __builtin_amdgcn_exp2f(sc[(NSU - 1) * 64] - M);
      l += p;
      VNEXT(0, p, 0) VNEXT(1, p, 0) VNEXT(2, p, 0) VNEXT(3, p, 0)
      if (wave == 0) STAMP_AT(2);
      USTAMP(15 + NSU);
      *((LAS float*)(sm + L_LS) + wave * 64 + lane) = l;
      dump(0);
    }
    for (int j = 1, second = 0; t < NITEMS;) {
      const __amdgpu_buffer_rsrc_t rc = rv;
      const int oc = on, half = nhalf, last = !ntwo;
      if (!second) pn = pl;
      if (__builtin_expect(!__all(((pn ^ tag) & 0x3fu) == 0u), 0)) {
        for (unsigned s = 0; s < SPIN_CAP; ++s) {
          __builtin_amdgcn_s_sleep(8);
          pn = __builtin_amdgcn_raw_buffer_load_b32(rw, pn_off + lane * 4, 0, 16);
          cfence();
          if (__all(((pn ^ tag) & 0x3fu) == 0u)) break;
        }
      }
      const float p = dec13(pn >> (half ? 19 : 6));
      VNEXT(0, p, j) VNEXT(1, p, j) VNEXT(2, p, j) VNEXT(3, p, j)
      second = !last;
      if (last) { dump(j); ++j; }
    }
    if (wave == 0) STAMP_AT(3);
    return;
  }

  const int hw = wid < 8 ? wid : wid - 8;
  u64 tag64 = 0;
  unsigned tag;
  if (hw < 4) {
    __builtin_amdgcn_s_setprio(3);
    int vzero;
    asm volatile("v_mov_b32 %0, 0" : "=v"(vzero));
    u64 epoch_v = __hip_atomic_load((GAS u64*)(ws + W_EPOCH + vzero), RLX_AGENT);
    __builtin_amdgcn_sched_barrier(0);
    const __amdgpu_buffer_rsrc_t rq = __builtin_amdgcn_make_buffer_rsrc((void*)wq, 0, D_ * HD * 2, 0x00020000);
    u32x4 w[8];
#pragma unroll
    for (int i = 0; i < 8; ++i) w[i] = __builtin_amdgcn_raw_buffer_load_b128(rq, r * 8192 + c * 16, (128 * bat + 32 * hw + 4 * i) * 8192 + head * 256, WQ_AUX);
    wg_barrier();
    asm volatile("" : "+v"(epoch_v));
#pragma unroll
    for (int i = 0; i < 8; ++i) *(LAS u32x4*)(sm + L_W + (c >> 1) * W_TS + rowpos(32 * hw + 4 * i + r) * 32 + (c & 1) * 16) = w[i];
    tag64 = (((u64)(unsigned)__builtin_amdgcn_readfirstlane((int)(epoch_v >> 32)) << 32) | (unsigned)__builtin_amdgcn_readfirstlane((int)epoch_v)) + 1;
    asm volatile("" :: "s"(tag64));
    tag = (unsigned)tag64 & 0xffu;
    if (hw == 0) STAMP_AT(12);
    wg_barrier();
    if (hw == 0) { ctl[C_TAG] = (int)tag; STAMP_AT(10); }
    f32x4 d0 = {0.f, 0.f, 0.f, 0.f}, d1 = d0;
#pragma unroll
    for (int s = 0; s < 4; ++s) {
      const bf16x8 xf = *(LAS bf16x8*)(sm + L_XN + (lane & 7) * XN_STR + (128 * bat + 32 * s + 8 * r) * 2);
      d0 = __builtin_amdgcn_mfma_f32_16x16x32_bf16(ld_bfrag(sm + L_W + (2 * hw) * W_TS, 32 * s, lane), xf, d0, 0, 0, 0);
      d1 = __builtin_amdgcn_mfma_f32_16x16x32_bf16(ld_bfrag(sm + L_W + (2 * hw + 1) * W_TS, 32 * s, lane), xf, d1, 0, 0, 0);
    }
    st_tag4<0xfu>(rw, W_QS + (((c & 7) * 32 + head) * 8 + bat) * 512 + (32 * hw + 2 * (c & 8) + 4 * r) * 4, (c & 8) ? d1 : d0, tag);
    lds_add(ctl + C_ST);
    if (hw == 0) STAMP_AT(11);
    for (int i = 0; i < Q_NAP; ++i) __builtin_amdgcn_s_sleep(4);
    if (hw == 0) { STAMP_AT(22); STAMP_VAL(30, 0); }
    u32x4 g = ld_tag4(rw, W_QS + (bat * 32 + head) * 4096 + hw * 1024 + lane * 16);
    __builtin_amdgcn_sched_barrier(0);
    const __amdgpu_buffer_rsrc_t ro = __builtin_amdgcn_make_buffer_rsrc((void*)wo, 0, HD * D_ * 2, 0x00020000);
    const __amdgpu_buffer_rsrc_t rd = __builtin_amdgcn_make_buffer_rsrc((void*)wd, 0, F_ * D_ * 2, 0x00020000);
    u32x4 wv[15];
#pragma unroll
    for (int i = 0; i < 15; ++i) {
      wv[i] = __builtin_amdgcn_raw_buffer_load_b128(i < 8 ? ro : rd, r * 2048 + c * 16, ((i < 8 ? 128 * head : 112 * head - 128) + 4 * hw + 16 * i) * 2048 + bat * 256, W_AUX);
      __builtin_amdgcn_sched_barrier(0);
    }
    if (__builtin_expect(!__all(tag_ok<0xfu>(g, tag)), 0)) {
      for (unsigned s = 0; s < SPIN_CAP; ++s) {
        g = ld_tag4(rw, W_QS + (bat * 32 + head) * 4096 + hw * 1024 + lane * 16);
        cfence();
        if (hw == 0) STAMP_VAL(30, s + 1);
        if (__all(tag_ok<0xfu>(g, tag))) break;
      }
    }
    if (hw == 0) STAMP_AT(23);
    *(LAS f32x4*)(sm + L_QP + hw * 1024 + lane * 16) = untag4<0xfu>(g);
    lds_add(ctl + C_Q);
    lds_wait_ge<0>(ctl + C_ST, 4);
#pragma unroll
    for (int i = 0; i < 15; ++i) *(LAS u32x4*)(sm + L_W + (c >> 1) * W_TS + rowpos(4 * hw + 16 * i + r) * 32 + (c & 1) * 16) = wv[i];
    lds_add(ctl + C_WC);
    if (hw == 3 && xcd == 0 && lane == 0) __hip_atomic_store((GAS u64*)(ws + W_CNT + grp * 256), tag64 << 16, RLX_AGENT);
    if (hw == 0) STAMP_AT(25);
    if (hw != 0) __builtin_amdgcn_s_setprio(H_PRIO);
  } else {
    const int gw = hw - 4;
    __builtin_amdgcn_s_setprio(H_PRIO);
    {
      const u32x4 xa = *(const u32x4*)(x + hw * D_ + lane * 8), xb = *(const u32x4*)(x + hw * D_ + 512 + lane * 8);
      wg_barrier();
      if (gw < 3) ctl[gw * 64 + lane] = gw ? -1 : 0;
      ln_row(xa, xb, sm + L_XN + hw * XN_STR, lane);
    }
    if (xcd != (grp & 7)) {
      const int fb = grp * 7 + xcd - (xcd > (grp & 7));
      const int cb = fb >> 2, ks = fb & 3, row0 = 256 * ks + 64 * gw;
      const char* wp = ((lane & 8) ? wu : wg) + (size_t)(row0 + 8 * r) * (F_ * 2) + cb * 128 + (lane & 7) * 16;
      u32x4 w[16];
#define GLOAD(i) { w[i] = GU_AUX ? __builtin_nontemporal_load((const u32x4*)(wp + (32 * ((i) >> 3) + ((i) & 7)) * (F_ * 2))) : *(const u32x4*)(wp + (32 * ((i) >> 3) + ((i) & 7)) * (F_ * 2)); __builtin_amdgcn_sched_barrier(0); }
      if (gw == 0) STAMP_AT(26);
#pragma unroll
      for (int i = 0; i < G_FIRST; ++i) GLOAD(i)
      wg_barrier();
      lds_wait_ge<0>(ctl + C_ST, 4);
#pragma unroll
      for (int i = G_FIRST; i < 16; ++i) GLOAD(i)
      tag = (unsigned)lds_ld(ctl + C_TAG);
      f32x4 d[8];
#pragma unroll
      for (int j = 0; j < 8; ++j) d[j] = f32x4{0.f, 0.f, 0.f, 0.f};
#pragma unroll
      for (int s = 0; s < 2; ++s) {
        const bf16x8 af = *(LAS bf16x8*)(sm + L_XN + (lane & 7) * XN_STR + (row0 + 32 * s + 8 * r) * 2);
#pragma unroll
        for (int j = 0; j < 8; ++j) {
          u32x4 bq;
#pragma unroll
          for (int mm = 0; mm < 4; ++mm) bq[mm] = __builtin_amdgcn_perm(w[8 * s + 2 * mm + 1][j >> 1], w[8 * s + 2 * mm][j >> 1], (j & 1) ? 0x07060302 : 0x05040100);
          d[j] = __builtin_amdgcn_mfma_f32_16x16x32_bf16(af, __builtin_bit_cast(bf16x8, bq), d[j], 0, 0, 0);
        }
      }
      {
        const int b0_ = 4 * (r & 1) + 2 * (r >> 1);
        const bool hi = r >> 1;
        LAS char* dst = sm + L_GUP + gw * 4096 + b0_ * 512 + c * 32;
#pragma unroll
        for (int e = 0; e < 2; ++e) {
          *(LAS f32x4*)(dst + e * 512) = f32x4{hi ? d[0][2 + e] : d[0][e], hi ? d[1][2 + e] : d[1][e], hi ? d[2][2 + e] : d[2][e], hi ? d[3][2 + e] : d[3][e]};
          *(LAS f32x4*)(dst + e * 512 + 16) = f32x4{hi ? d[4][2 + e] : d[4][e], hi ? d[5][2 + e] : d[5][e], hi ? d[6][2 + e] : d[6][e], hi ? d[7][2 + e] : d[7][e]};
        }
      }
      lds_add(ctl + C_GU);
      lds_wait_ge<1>(ctl + C_GU, 4);
      {
        LAS char* src = sm + L_GUP + ((lane >> 2) & 7) * 512 + (lane >> 5) * 256 + gw * 64 + (lane & 3) * 16;
        const f32x4 sum = (*(LAS f32x4*)src + *(LAS f32x4*)(src + 4096)) + (*(LAS f32x4*)(src + 8192) + *(LAS f32x4*)(src + 12288));
        st_tag4(rw, W_GS + ((4 * cb + gw) * 4 + ks) * 1024 + lane * 16, sum, tag);
      }
      if (gw == 3) STAMP_AT(28);
      if (gw == 1) {
        lds_wait_ge<32>(ctl + C_BLK, WAIT_G2);
        nap((blockIdx.x * 5) & (STAGGER - 1));
        u32x4 g[4];
        for (unsigned s = 0; s < SPIN_CAP; ++s) {
#pragma unroll
          for (int i = 0; i < 4; ++i) g[i] = ld_tag4(rw,
            W_GS + fb * 4096 + i * 1024 + lane * 16);
          cfence();
          if (__all(tag_ok(g[0], tag) && tag_ok(g[1], tag) && tag_ok(g[2], tag) && tag_ok(g[3], tag))) break;
          __builtin_amdgcn_s_sleep(32);
        }
        const f32x4 sv = (untag4(g[0]) + untag4(g[1])) + (untag4(g[2]) + untag4(g[3]));
        f32x4 h;
#pragma unroll
        for (int k = 0; k < 4; ++k) {
          auto t = __builtin_amdgcn_permlane32_swap(__float_as_uint(sv[k]), __float_as_uint(sv[k]), false, false);
          h[k] = rbf(rbf(__uint_as_float(t[0])) * rbf(__uint_as_float(t[1])));
        }
        st_tag4(rw, lane < 32 ? W_HG + (fb / 7) * 4096 + (lane >> 2) * 512 + (fb % 7) * 64 + (lane & 3) * 16 : OOB, h, tag);
        STAMP_AT(4);
      }
    } else {
      wg_barrier();
      lds_wait_ge<0>(ctl + C_ST, 4);
      tag = (unsigned)lds_ld(ctl + C_TAG);
    }
  }
  if (hw == 2) {
    lds_wait_ge<32>(ctl + C_BLK, WAIT_H);
    nap((blockIdx.x * 3) & (STAGGER - 1));
    for (unsigned s = 0; s < SPIN_CAP; ++s) {
      u32x4 g[4];
      const bool live = (lane & 31) < 28;
#pragma unroll
      for (int i = 0; i < 4; ++i) g[i] = ld_tag4(rw, live ? W_HG + head * 4096 + i * 1024 + lane * 16 : OOB);
      cfence();
#pragma unroll
      for (int i = 0; i < 4; ++i) *(LAS u32x2*)(sm + L_HA + (2 * i + (lane >> 5)) * A_STR + (lane & 31) * 8) = hi16x4(g[i]);
      if (__all(!live || (tag_ok(g[0], tag) && tag_ok(g[1], tag) && tag_ok(g[2], tag) && tag_ok(g[3], tag)))) break;
      __builtin_amdgcn_s_sleep(64);
    }
    lds_add(ctl + C_WC);
  }
  f32x4 dacc[2] = {{0.f, 0.f, 0.f, 0.f}, {0.f, 0.f, 0.f, 0.f}};
  bf16x8 bo[2][4];
  if (hw >= 4) {
    lds_wait_ge<16>(ctl + C_WC, 5);
#pragma unroll
    for (int tt = 0; tt < 2; ++tt) {
      LAS char* img = sm + L_W + (2 * (hw - 4) + tt) * W_TS;
#pragma unroll
      for (int s = 0; s < 4; ++s) bo[tt][s] = ld_bfrag(img, 32 * s, lane);
#pragma unroll
      for (int s = 0; s < 3; ++s)
        dacc[tt] = __builtin_amdgcn_mfma_f32_16x16x32_bf16(ld_bfrag(img, 128 + 32 * s, lane), *(LAS bf16x8*)(sm + L_HA + (lane & 7) * A_STR + (32 * s + 8 * r) * 2), dacc[tt], 0, 0, 0);
      dacc[tt] = __builtin_amdgcn_mfma_f32_16x16x16bf16_1k(ld_tr(img + rowpos(224 + 4 * r + ((lane >> 2) & 3)) * 32 + (lane & 3) * 8),
                                                           *(LAS s16x4*)(sm + L_HA + (lane & 7) * A_STR + (96 + 4 * r) * 2), dacc[tt], 0, 0, 0);
    }
  }
  GAS u64* const cnt = (GAS u64*)(ws + W_CNT + grp * 256);
  if (hw == 0) {
    lds_wait_ge<32>(ctl + C_BLK, 13 + NSU);
    int last = 0;
    for (int got = 0;; ++got) {
      for (unsigned s = 0; s < (SPIN_CAP << 2); ++s) {
        const int p0 = lds_ld(ctl + C_P), p1 = lds_ld(ctl + C_P + 1), p2 = lds_ld(ctl + C_P + 2), p3 = lds_ld(ctl + C_P + 3);
        if (max(max(p0, p1), max(p2, p3)) + (last < LOOK2 ? 1 : 0) > got && max(max(p0, p1), max(p2, p3)) > 0 && min(min(p0, p1), min(p2, p3)) >= got - SKEW - (last < LOOK2 && SKEW < 1 ? 1 : 0) && lds_ld(ctl + C_PUB) >= got - 2) break;
        __builtin_amdgcn_s_sleep(PROG_SLEEP);
      }
      int t = NITEMS;
      for (unsigned s = 0; s < SPIN_CAP; ++s) {
        u64 v = 0;
        if (lane == 0) v = __hip_atomic_fetch_add(cnt, 1ull, RLX_AGENT) ^ (tag64 << 16);
        const unsigned lo = __builtin_amdgcn_readfirstlane((unsigned)v), hi = __builtin_amdgcn_readfirstlane((unsigned)(v >> 32));
        if (hi == 0u && lo < 65536u) { t = (int)lo; break; }
        __builtin_amdgcn_s_sleep(32);
      }
      if (lane == 0) __hip_atomic_store(ctl + C_TICK + got, t, RLX_WG);
      last = t;
      if (t >= NITEMS || got >= NITEMS) break;
    }
    STAMP_AT(5);
  } else {
    if (hw >= 4) {
      lds_wait_ge<16>(ctl + C_M, 4);
      const f32x4 mw = *(LAS f32x4*)(ctl + C_MX);
      const float M = fmaxf(fmaxf(mw[0], mw[1]), fmaxf(mw[2], mw[3]));
      LAS float* src = (LAS float*)(sm + L_SC) + (hw - 4) * 1024 + min(NSU + 2 * r, 14) * 64 + 4 * c;
      const f32x4 s0 = *(LAS f32x4*)src, s1 = *(LAS f32x4*)(src + 64);
      u32x4 pw;
      float l = 0.f;
#pragma unroll
      for (int k = 0; k < 4; ++k) {
        const unsigned e0 = enc13(__builtin_amdgcn_exp2f(s0[k] - M)), e1 = enc13(__builtin_amdgcn_exp2f(s1[k] - M));
        pw[k] = (e0 << 6) | (e1 << 19) | (tag & 0x3fu);
        l += dec13(e0) + dec13(e1);
      }
      {
        const u64 tg = (__builtin_amdgcn_s_memrealtime() | ((1u << P_GRID) - 1)) + 1;
        for (unsigned s = 0; s < SPIN_CAP && __builtin_amdgcn_s_memrealtime() < tg; ++s) __builtin_amdgcn_s_sleep(1);
      }
      __builtin_amdgcn_raw_buffer_store_b128(pw, rw, r < NP ? W_P + (((bat * 32 + head) * NP + r) * 4 + hw - 4) * 256 + c * 16 : OOB, 0, 16);
      *(LAS float*)(ctl + C_LD + hw - 4) = wave_sum(r < NP ? l : 0.f);
      lds_add(ctl + C_PL);
    }
    if (hw == 1) {
      lds_wait_ge<32>(ctl + C_BLK, 14 + NSU);
      __builtin_amdgcn_s_setprio(3);
      for (int j = 0, t = 0;; ++j) {
        lds_wait_ge<REC_SLEEP>(ctl + C_CNT + (j & 3), 4 * ((j >> 2) + 1));
        LAS char* src = sm + L_DUMP + (j & 3) * 8192 + (lane >> 5) * 4096 + (lane & 1) * 1024 + ((lane & 31) >> 1) * 16;
        f32x4 o0 = (*(LAS f32x4*)src + *(LAS f32x4*)(src + 256)) + (*(LAS f32x4*)(src + 512) + *(LAS f32x4*)(src + 768));
        f32x4 o1 = (*(LAS f32x4*)(src + 2048) + *(LAS f32x4*)(src + 2304)) + (*(LAS f32x4*)(src + 2560) + *(LAS f32x4*)(src + 2816));
        int off = W_REC + (bat * 32 + head) * REC_ROW + (NSLOT - 1) * 512 + lane * 16;
        float L = 0.f;
        if (j == 0) {
          lds_wait_ge<1>(ctl + C_PL, 4);
          const f32x4 mw = *(LAS f32x4*)(ctl + C_MX), ld = *(LAS f32x4*)(ctl + C_LD);
          const float M = fmaxf(fmaxf(mw[0], mw[1]), fmaxf(mw[2], mw[3]));
          f32x4 e;
#pragma unroll
          for (int w = 0; w < 4; ++w) { e[w] = __builtin_amdgcn_exp2f(mw[w] - M); L = fmaf(e[w], *((LAS float*)(sm + L_LS) + w * 64 + lane), L); }
          L = wave_sum(L) + ((ld[0] + ld[1]) + (ld[2] + ld[3]));
          o0 *= (lane >> 5) ? e[2] : e[0]; o1 *= (lane >> 5) ? e[3] : e[1];
        } else {
          const Item it = item_of(t);
          off = W_REC + (bat * 32 + 4 * it.rowi + (grp & 3)) * REC_ROW + it.slot * 512 + lane * 16;
        }
        o0 += o1;
#pragma unroll
        for (int k = 0; k < 4; ++k) o0[k] = x32_sum(o0[k]);
        const float L_lo = L - untag(tagw(L, 0));
        if (lane >= 32) { o0 = f32x4{L, L_lo, L, L_lo}; off = (lane == 32 && j == 0) ? off : OOB; }
        st_tag4(rw, off, o0, tag);
        lgkm0();
        __hip_atomic_store(ctl + C_PUB, j + 1, RLX_WG);
        t = lds_ld(ctl + C_TICK + j);
        if ((unsigned)t >= (unsigned)NITEMS) break;
      }
      __hip_atomic_store(ctl + C_SDONE, 4, RLX_WG);
    }
  }
  const int rchunk = blockIdx.x >> 3, rb = blockIdx.x & 7;
  u32x2 xres = {0u, 0u};
  if (hw == 0) xres = *(const u32x2*)(x + rb * D_ + rchunk * 32 + (lane & 7) * 4);
  if (hw == 0) STAMP_AT(6);
  lds_wait_ge<1>(ctl + C_SDONE, 4);
  if (hw == 0) STAMP_AT(7);
  {
    constexpr int NL = NSLOT / 2 + 1;
    u32x4 g[NL];
    int off[NL];
#pragma unroll
    for (int i = 0; i < NL; ++i) off[i] = i * 1024 + lane * 16 <= NSLOT * 512 ? W_REC + (hw * 32 + head) * REC_ROW + i * 1024 + lane * 16 : OOB;
    gather<NL>(rw, off, g, tag, lane);
    const int ll = (NSLOT & 1) * 32;
    const float inv = 1.f / (__uint_as_float(__builtin_amdgcn_readlane(g[NL - 1][0], ll) & 0xffffff00u) + __uint_as_float(__builtin_amdgcn_readlane(g[NL - 1][1], ll) & 0xffffff00u));
    f32x4 o = untag4(g[0]);
#pragma unroll
    for (int i = 1; i < NL - 1; ++i) o += untag4(g[i]);
    if ((NSLOT & 1) && lane < 32) o += untag4(g[NL - 1]);
#pragma unroll
    for (int k = 0; k < 4; ++k) o[k] = x32_sum(o[k]) * inv;
    *(LAS u32x2*)(sm + L_OA + hw * A_STR + (lane & 31) * 8) = u32x2{pack2(o[0], o[1]), pack2(o[2], o[3])};
  }
  lds_inc(ctl + C_OA);
  if (hw >= 4) {
    lds_wait_ge<0>(ctl + C_OA, 8);
    if (hw == 4) STAMP_AT(8);
#pragma unroll
    for (int s = 0; s < 4; ++s) {
      const bf16x8 of = *(LAS bf16x8*)(sm + L_OA + (lane & 7) * A_STR + (32 * s + 8 * r) * 2);
      dacc[0] = __builtin_amdgcn_mfma_f32_16x16x32_bf16(bo[0][s], of, dacc[0], 0, 0, 0);
      dacc[1] = __builtin_amdgcn_mfma_f32_16x16x32_bf16(bo[1][s], of, dacc[1], 0, 0, 0);
    }
    st_tag4(rw, W_PART + ((4 * bat + hw - 4) * 32 + head) * 1024 + (c & 7) * 128 + (2 * (c & 8) + 4 * r) * 4, (c & 8) ? dacc[1] : dacc[0], tag);
  }
  if (hw != 0) return;
  {
    u32x4 g[4];
    int off[4];
#pragma unroll
    for (int k = 0; k < 4; ++k) off[k] = W_PART + (rchunk * 32 + 8 * k + (lane >> 3)) * 1024 + rb * 128 + (lane & 7) * 16;
    gather<4>(rw, off, g, tag, lane);
    f32x4 sum = (untag4(g[0]) + untag4(g[1])) + (untag4(g[2]) + untag4(g[3]));
#pragma unroll
    for (int k = 0; k < 4; ++k) { float t = sum[k]; t += dpp<XOR8>(t); sum[k] = xrow_sum(t); }
    if (lane < 8)
      *(u32x2*)(out + rb * D_ + rchunk * 32 + lane * 4) = u32x2{pack2(bflo(xres[0]) + sum[0], bfhi(xres[0]) + sum[1]), pack2(bflo(xres[1]) + sum[2], bfhi(xres[1]) + sum[3])};
    if (blockIdx.x == 0 && lane == 0) __hip_atomic_store((GAS u64*)(ws + W_EPOCH), tag64, RLX_AGENT);
    STAMP_AT(9);
    STAMP_DUMP();
  }
}

extern "C" void kernel_launch(void* const* d_in, const int* in_sizes, int n_in, void* d_out, int out_size, void* d_ws, size_t ws_size,
                              hipStream_t stream) {
  fused_decode<<<256, NTHR, 0, stream>>>((const unsigned short*)d_in[0], (const char*)d_in[1], (const char*)d_in[2], (const char*)d_in[3],
                                         (const char*)d_in[4], (const char*)d_in[6], (const char*)d_in[7], (const char*)d_in[8],
                                         (unsigned short*)d_out, (char*)d_ws + ((ws_size - (8u << 20)) & ~(size_t)4095));
}
```
